```python
import torch

def _softmax(x, axis=-1):
    return torch.softmax(x, dim=axis)
def _gelu(x):
    return torch.nn.functional.gelu(x, approximate="tanh")
def _silu(x):
    return torch.nn.functional.silu(x)
def _sigmoid(x):
    return torch.sigmoid(x)
def _layernorm(x, axis=-1, eps=1e-5):
    m = x.mean(dim=axis, keepdim=True)
    v = x.var(dim=axis, keepdim=True, unbiased=False)
    return (x - m) / torch.sqrt(v + eps)
def _scatter_add(tgt, idx, src, axis=0):
    return tgt.clone().index_add_(axis, idx, src)
def _segment_sum(src, seg, n):
    out = torch.zeros((n,) + src.shape[1:], dtype=src.dtype)
    return out.index_add_(0, seg, src)
def _pad(x, pw):
    flat = tuple(v for pair in reversed(pw) for v in pair)
    return torch.nn.functional.pad(x, flat)

kernel_name = 'concat_heads__grouped_kv_mqa__compressed_kv_cla__prompt'

def setup_inputs():

    _w = (lambda a: (lambda t: t.to(torch.bfloat16) if t.is_floating_point() else t)(torch.as_tensor(a)))
    def _A_setup():
        B, S, D, H = 1, 2048, 576, 32
        Dh = D // H
        return {
            "x": _w((lambda *s: torch.randn(*s, dtype=torch.bfloat16) * 0.05)(B, S, D)),
            "wq": _w((lambda *s: torch.randn(*s, dtype=torch.bfloat16) * 0.8)(D, H * Dh)),
            "wk": _w((lambda *s: torch.randn(*s, dtype=torch.bfloat16) * 0.8)(D, Dh)),
            "wv": _w((lambda *s: torch.randn(*s, dtype=torch.bfloat16) * 0.05)(D, Dh)),
            "wo": _w((lambda *s: torch.randn(*s, dtype=torch.bfloat16) * 0.05)(H * Dh, D)),
        }
    _A_in = _A_setup()

    def _B_setup():
        B, H, Sq, Sk, D, M = 1, 32, 2048, 2048, 128, 576
        Dc = 4 * D
        return {
            "x": _w((lambda *s: torch.randn(*s, dtype=torch.bfloat16) * 3.2)(B, Sq, M)),
            "c_kv": _w((lambda *s: torch.randn(*s, dtype=torch.bfloat16) * 0.1)(B, Sk, Dc)),
            "w_q": _w((lambda *s: torch.randn(*s, dtype=torch.bfloat16) * 6.4)(M, H, D) * (M ** -0.5)),
            "w_uk": _w((lambda *s: torch.randn(*s, dtype=torch.bfloat16) * 6.4)(Dc, H, D) * (Dc ** -0.5)),
            "w_uv": _w((lambda *s: torch.randn(*s, dtype=torch.bfloat16) * 0.1)(Dc, H, D) * (Dc ** -0.5)),
            "w_o": _w((lambda *s: torch.randn(*s, dtype=torch.bfloat16) * 0.1)(H, D, M) * ((H * D) ** -0.5)),
        }
    _B_in = _B_setup()

    return {
        "A_x": _A_in["x"],
        "A_wq": _A_in["wq"],
        "A_wk": _A_in["wk"],
        "A_wv": _A_in["wv"],
        "A_wo": _A_in["wo"],
        "B_x": _B_in["x"],
        "B_c_kv": _B_in["c_kv"],
        "B_w_q": _B_in["w_q"],
        "B_w_uk": _B_in["w_uk"],
        "B_w_uv": _B_in["w_uv"],
        "B_w_o": _B_in["w_o"],
    }

def reference(A_x, A_wq, A_wk, A_wv, A_wo, B_x, B_c_kv, B_w_q, B_w_uk, B_w_uv, B_w_o):
    def _A_ref(x, wq, wk, wv, wo):
        B, S, D = x.shape
        H = 32
        Dh = D // H
        q = (lambda x, *d: x.permute(*d))((x @ wq).reshape(B, S, H, Dh), 0, 2, 1, 3)
        k = x @ wk
        v = x @ wv
        scores = torch.einsum("bhqd,bkd->bhqk", q, k) * (Dh ** -0.5)
        p = _softmax(scores, axis=-1)
        o = torch.einsum("bhqk,bkd->bhqd", p, v)
        return (lambda x, *d: x.permute(*d))(o, 0, 2, 1, 3).reshape(B, S, H * Dh) @ wo

    def _B_ref(x, c_kv, w_q, w_uk, w_uv, w_o):
        B, Sq, M = x.shape[0], x.shape[1], x.shape[2]
        Sk = c_kv.shape[1]
        H, D = w_q.shape[1], w_q.shape[2]
        q = torch.einsum("bqm,mhd->bhqd", x, w_q)
        k = torch.einsum("bkc,chd->bhkd", c_kv, w_uk)
        v = torch.einsum("bkc,chd->bhkd", c_kv, w_uv)
        scores = torch.einsum("bhqd,bhkd->bhqk", q, k) * (D ** -0.5)
        off = Sk - Sq
        qi = torch.arange(Sq)[:, None]
        kj = torch.arange(Sk)[None, :]
        cmask = kj <= (qi + off)
        scores = torch.where(cmask[None, None], scores, -1e9)
        p = _softmax(scores, axis=-1)
        ctx = torch.einsum("bhqk,bhkd->bhqd", p, v)
        return torch.einsum("bhqd,hdm->bqm", ctx, w_o)

    a = _A_ref(A_x, A_wq, A_wk, A_wv, A_wo)
    b = _B_ref(B_x, B_c_kv, B_w_q, B_w_uk, B_w_uv, B_w_o)
    return torch.concatenate([a, b], axis=-1)
```

```cpp
#include <hip/hip_runtime.h>
#include <hip/hip_bf16.h>
#include <stdint.h>
typedef unsigned short u16;
__device__ __forceinline__ float bf2f(u16 b) { return __uint_as_float(((unsigned)b) << 16); }
__device__ __forceinline__ u16 f2bf(float f) { unsigned u = __float_as_uint(f); u += 0x7FFFu + ((u >> 16) & 1u); return (u16)(u >> 16); }

__global__ void gemm_naive(const u16* A, int lda, const u16* B, int ldb, u16* C, int ldc, int M, int N, int K) {
  __shared__ float As[16][17], Bs[16][17];
  int tx = threadIdx.x, ty = threadIdx.y;
  int m = blockIdx.y * 16 + ty, n = blockIdx.x * 16 + tx;
  float acc = 0.f;
  for (int k0 = 0; k0 < K; k0 += 16) {
    As[ty][tx] = (m < M && k0 + tx < K) ? bf2f(A[(size_t)m * lda + k0 + tx]) : 0.f;
    Bs[ty][tx] = (k0 + ty < K && n < N) ? bf2f(B[(size_t)(k0 + ty) * ldb + n]) : 0.f;
    __syncthreads();
    for (int k = 0; k < 16; ++k) acc += As[ty][k] * Bs[k][tx];
    __syncthreads();
  }
  if (m < M && n < N) C[(size_t)m * ldc + n] = f2bf(acc);
}
__global__ void attn_naive(const u16* Q, int ldq, const u16* Kp, int ldk, const u16* Vp, int ldv, u16* O, int ldo, int S, int D, int kvstride, float scale, int causal) {
  __shared__ float sc[2048]; __shared__ float qs[128]; __shared__ float red[256];
  int q = blockIdx.x, h = blockIdx.y, t = threadIdx.x;
  if (t < D) qs[t] = bf2f(Q[(size_t)q * ldq + h * D + t]);
  __syncthreads();
  int nk = causal ? q + 1 : S;
  float mx = -1e30f;
  for (int k = t; k < nk; k += 256) {
    float s = 0.f; const u16* kp = Kp + (size_t)k * ldk + h * kvstride;
    for (int d = 0; d < D; ++d) s += qs[d] * bf2f(kp[d]);
    s *= scale; sc[k] = s; mx = fmaxf(mx, s);
  }
  red[t] = mx; __syncthreads();
  for (int o = 128; o > 0; o >>= 1) { if (t < o) red[t] = fmaxf(red[t], red[t + o]); __syncthreads(); }
  mx = red[0]; __syncthreads();
  float sum = 0.f;
  for (int k = t; k < nk; k += 256) { float p = __expf(sc[k] - mx); sc[k] = p; sum += p; }
  red[t] = sum; __syncthreads();
  for (int o = 128; o > 0; o >>= 1) { if (t < o) red[t] += red[t + o]; __syncthreads(); }
  sum = red[0];
  if (t < D) {
    float acc = 0.f;
    for (int k = 0; k < nk; ++k) acc += sc[k] * bf2f(Vp[(size_t)k * ldv + h * kvstride + t]);
    O[(size_t)q * ldo + h * D + t] = f2bf(acc / sum);
  }
}
extern "C" void kernel_launch(void* const* d_in, const int* in_sizes, int n_in, void* d_out, int out_size, void* d_ws, size_t ws_size, hipStream_t stream) {
  const u16 *Ax = (const u16*)d_in[0], *Awq = (const u16*)d_in[1], *Awk = (const u16*)d_in[2], *Awv = (const u16*)d_in[3], *Awo = (const u16*)d_in[4];
  const u16 *Bx = (const u16*)d_in[5], *Bc = (const u16*)d_in[6], *Bwq = (const u16*)d_in[7], *Bwuk = (const u16*)d_in[8], *Bwuv = (const u16*)d_in[9], *Bwo = (const u16*)d_in[10];
  u16* out = (u16*)d_out; u16* ws = (u16*)d_ws;
  const int S = 2048;
  u16* qA = ws; u16* kA = qA + (size_t)S * 576; u16* vA = kA + (size_t)S * 32; u16* oA = vA + (size_t)S * 32;
  u16* qB = oA + (size_t)S * 576; u16* kB = qB + (size_t)S * 4096; u16* vB = kB + (size_t)S * 4096; u16* oB = vB + (size_t)S * 4096;
  dim3 b(16, 16);
  auto G = [&](const u16* A, int lda, const u16* B, int ldb, u16* C, int ldc, int M, int N, int K) {
    gemm_naive<<<dim3((N + 15) / 16, (M + 15) / 16), b, 0, stream>>>(A, lda, B, ldb, C, ldc, M, N, K); };
  G(Ax, 576, Awq, 576, qA, 576, S, 576, 576);
  G(Ax, 576, Awk, 18, kA, 18, S, 18, 576);
  G(Ax, 576, Awv, 18, vA, 18, S, 18, 576);
  attn_naive<<<dim3(S, 32), 256, 0, stream>>>(qA, 576, kA, 18, vA, 18, oA, 576, S, 18, 0, 0.23570226f, 0);
  G(oA, 576, Awo, 576, out, 1152, S, 576, 576);
  G(Bx, 576, Bwq, 4096, qB, 4096, S, 4096, 576);
  G(Bc, 512, Bwuk, 4096, kB, 4096, S, 4096, 512);
  G(Bc, 512, Bwuv, 4096, vB, 4096, S, 4096, 512);
  attn_naive<<<dim3(S, 32), 256, 0, stream>>>(qB, 4096, kB, 4096, vB, 4096, oB, 4096, S, 128, 128, 0.088388348f, 1);
  G(oB, 4096, Bwo, 576, out + 576, 1152, S, 576, 4096);
}
```

```cpp
namespace g1 {
template <int NN>
DI void qtile(LAS char* lds, const u16* __restrict__ X, const u16* __restrict__ wq, u16* __restrict__ QA, int ti, int tj) {
  const int tid = threadIdx.x, wid = __builtin_amdgcn_readfirstlane(tid >> 6), lane = tid & 63, wm = wid & 3, fr = lane & 15, g = lane >> 4;
  constexpr int NB0 = NN == 5 ? 0 : 5;
  f32x4 acc[2][5];
#pragma unroll
  for (int m = 0; m < 2; ++m)
#pragma unroll
    for (int n = 0; n < 5; ++n) acc[m][n] = (f32x4){0.f, 0.f, 0.f, 0.f};
  outp::kloop<false, NN>(acc, lds, (const char*)X, wq, ti, tj, 0, 9);
#pragma unroll
  for (int m = 0; m < 2; ++m)
#pragma unroll
    for (int n = 0; n < NN; ++n) { const f32x4 v = acc[m][n] * QSCALE_A;
      *(u32x2*)(QA + (size_t)(ti * 128 + 32 * wm + 16 * m + fr) * 576 + tj * 144 + 16 * (NB0 + n) + 4 * g) = (u32x2){cvtpk(v[0], v[1]), cvtpk(v[2], v[3])}; }
}
DI void kvtile(LAS char* lds, const u16* __restrict__ X, const u16* __restrict__ wk, const u16* __restrict__ wv, char* __restrict__ KA, char* __restrict__ VA, int ti) {
  const int tid = threadIdx.x, wid = __builtin_amdgcn_readfirstlane(tid >> 6), lane = tid & 63, fr = lane & 15, g = lane >> 4, q = (lane & 15) >> 2, p = lane & 3;
  const unsigned lds0 = (unsigned)(uintptr_t)lds;
  const __amdgpu_buffer_rsrc_t srdA = __builtin_amdgcn_make_buffer_rsrc((void*)X, (short)0, S * 1152, 0x00020000);
  { const int row = 8 * wid + (lane >> 3);
    const unsigned off = (unsigned)((ti * 64 + row) * 1152 + (((lane & 7) ^ ((row >> 1) & 7)) * 16));
#pragma unroll
    for (int kt = 0; kt < 9; ++kt) attb::dma16(srdA, off, kt * 128u, lds0 + kt * 8192u + wid * 1024u); }
  LAS char* Ws = lds + 73728;
  { unsigned v[27];
#pragma unroll
    for (int j = 0; j < 27; ++j) { const int i = tid + 512 * j, k = i / 24, c = i - 24 * k;
      const unsigned* p_ = c < 9 ? (const unsigned*)wk + k * 9 + c : (const unsigned*)wv + k * 9 + (c < 18 ? c - 9 : 0);
      v[j] = *p_; if (c >= 18) v[j] = 0u; }
#pragma unroll
    for (int j = 0; j < 27; ++j) { const int i = tid + 512 * j, k = i / 24, c = i - 24 * k; *(LAS unsigned*)(Ws + k * 96 + c * 4) = v[j]; } }
  f32x4 acc[3];
#pragma unroll
  for (int n = 0; n < 3; ++n) acc[n] = (f32x4){0.f, 0.f, 0.f, 0.f};
  const int row = 16 * (wid & 3) + fr;
  asm volatile("s_waitcnt vmcnt(0) lgkmcnt(0)\n\ts_barrier" ::: "memory");
  if (wid >= 4) return;
#pragma unroll 2
  for (int s = 0; s < 18; ++s) {
    const bf16x8 xf = *(const LAS bf16x8*)(lds + (s >> 1) * 8192 + row * 128 + (((4 * (s & 1) + g) ^ ((row >> 1) & 7)) * 16));
#pragma unroll
    for (int nb = 0; nb < 3; ++nb) {
      LAS const char* wp = Ws + (32 * s + 8 * g + q) * 96 + nb * 32 + p * 8;
      acc[nb] = MFMA16(cat8(tr_read(wp), tr_read(wp + 4 * 96)), xf, acc[nb]);
    }
  }
  const int key = ti * 64 + row;
#pragma unroll
  for (int nb = 0; nb < 3; ++nb)
#pragma unroll
    for (int r = 0; r < 4; ++r) {
      const int c = 16 * nb + 4 * g + r; const u16 val = (u16)(cvtpk(acc[nb][r], 0.f) & 0xffff);
      if (c < 18) { char* p = c < 8 ? KA + key * 16 + c * 2 : (c < 16 ? KA + 32768 + key * 16 + (c - 8) * 2 : KA + 65536 + key * 4 + (c - 16) * 2); *(u16*)p = val; }
      else if (c <= 36) {
        const int d = c - 18, w16 = key & 15, chunk = 2 * (key >> 4) + ((w16 >> 2) & 1), j = (w16 >> 3) * 4 + (w16 & 3);
        *(u16*)(VA + chunk * 304 + d * 16 + j * 2) = c == 36 ? (u16)0x3F80 : val;
      }
    }
}
}

__global__ __launch_bounds__(512, 2) void k1_proj(const u16* __restrict__ Ckv, const u16* __restrict__ Wuk, const u16* __restrict__ Wuv,
                                                  const u16* __restrict__ XA, const u16* __restrict__ wq, const u16* __restrict__ wk, const u16* __restrict__ wv, char* __restrict__ ws) {
  extern __shared__ __attribute__((aligned(16))) char smem[];
  LAS char* lds = (LAS char*)smem;
  const int b = blockIdx.x;
  CLK0();
  if (b < 256) {
    if (threadIdx.x == 0) ((unsigned*)(ws + WS_CNT))[b * 64] = 0u;
    const int xcd = b & 7, idx = b >> 3, pm = idx & 7, j = idx >> 3;
    const int pnl = 2 * xcd + (j & 1);
    if (j < 2) g3::kv_tile<true>(lds, Ckv, Wuk, pm, pnl, ws + WS_KV);
    else       g3::kv_tile<false>(lds, Ckv, Wuv, pm, pnl, ws + WS_KV);
  } else if (b < 256 + 32) {
    g1::kvtile(lds, XA, wk, wv, ws + WS_KA, ws + WS_VA, b - 256);
  } else {
    const int t = b - 288, ti = 2 * (t & 7) + ((t >> 3) & 1), tj = t >> 4;
    if (__builtin_amdgcn_readfirstlane(threadIdx.x >> 8) == 0) g1::qtile<5>(lds, XA, wq, (u16*)(ws + WS_QA), ti, tj);
    else g1::qtile<4>(lds, XA, wq, (u16*)(ws + WS_QA), ti, tj);
  }
  CLK1(ws, 0);
}
__global__ __launch_bounds__(512, 2) void k2_attn_onlyA(char* __restrict__ ws) {
  extern __shared__ __attribute__((aligned(16))) char smem[];
  LAS char* lds = (LAS char*)smem;
  const int t = blockIdx.x;
  CLK0();
  atta::run(lds, (const u16*)(ws + WS_QA), ws + WS_KA, (u16*)(ws + WS_OA), t & 31, t >> 5);
  CLK1(ws, 2);
}
__global__ __launch_bounds__(512, 2) void k2_attn_onlyB(const u16* __restrict__ XB, const u16* __restrict__ WqB, char* __restrict__ ws) {
  extern __shared__ __attribute__((aligned(16))) char smem[];
  LAS char* lds = (LAS char*)smem;
  const int b = blockIdx.x;
  const int xcd = b & 7, idx = b >> 3;
  CLK0();
  attb::run(lds, XB, WqB, ws + WS_KV, ws + WS_CTX, 4 * xcd + (idx >> 3), idx & 7);
  CLK1(ws, 1);
}
extern "C" void kernel_launch(void* const* d_in, const int* in_sizes, int n_in, void* d_out, int out_size, void* d_ws, size_t ws_size, hipStream_t stream) {
  const u16 *Ax = (const u16*)d_in[0], *Awq = (const u16*)d_in[1], *Awk = (const u16*)d_in[2], *Awv = (const u16*)d_in[3], *Awo = (const u16*)d_in[4];
  const u16 *Bx = (const u16*)d_in[5], *Bc = (const u16*)d_in[6], *Bwq = (const u16*)d_in[7], *Bwuk = (const u16*)d_in[8], *Bwuv = (const u16*)d_in[9], *Bwo = (const u16*)d_in[10];
  char* ws = (char*)d_ws;
  static bool init = false;
  if (!init) {
    (void)hipFuncSetAttribute((const void*)k1_proj, hipFuncAttributeMaxDynamicSharedMemorySize, outp::LDS_BYTES);
    (void)hipFuncSetAttribute((const void*)k2_attn_onlyA, hipFuncAttributeMaxDynamicSharedMemorySize, 160 * 1024);
    (void)hipFuncSetAttribute((const void*)k2_attn_onlyB, hipFuncAttributeMaxDynamicSharedMemorySize, 160 * 1024);
    (void)hipFuncSetAttribute((const void*)k3_out, hipFuncAttributeMaxDynamicSharedMemorySize, outp::LDS_BYTES);
    init = true; }
  k1_proj<<<256 + 96, 512, outp::LDS_BYTES, stream>>>(Bc, Bwuk, Bwuv, Ax, Awq, Awk, Awv, ws);
  k2_attn_onlyB<<<256, 512, 160 * 1024, stream>>>(Bx, Bwq, ws);
  k2_attn_onlyA<<<256, 512, 160 * 1024, stream>>>(ws);
  k3_out<<<256, 512, outp::LDS_BYTES, stream>>>(ws, Bwo, Awo, (u16*)d_out);
}
```

```cpp
namespace g1b {
struct Ctx {
  __amdgpu_buffer_rsrc_t srdX, srdW; unsigned xoff[2], woff[2], wstep; int xrd[2], wrd; unsigned ldsb; f32x16 acc; int row0, tn;
};
DI void init(Ctx& g, unsigned ldsb, int id, int w4, int lane, const u16* __restrict__ X, const u16* __restrict__ wq, const u16* __restrict__ wkv) {
  const int r32 = lane & 31, hi = lane >> 5, wm = w4 & 1, wn = w4 >> 1;
  g.row0 = 64 * (id & 31); g.tn = id >> 5; g.ldsb = ldsb;
  g.srdX = __builtin_amdgcn_make_buffer_rsrc((void*)X, (short)0, S * 1152, 0x00020000);
  const bool kv = g.tn == 9; const unsigned pitch = kv ? 128u : 1152u;
  g.srdW = __builtin_amdgcn_make_buffer_rsrc((void*)(kv ? wkv : wq + g.tn * 64), (short)0, kv ? 576 * 128 : 576 * 1152 - g.tn * 128, 0x00020000);
  g.wstep = 64u * pitch;
#pragma unroll
  for (int i = 0; i < 2; ++i) { const int row = 8 * (2 * w4 + i) + (lane >> 3);
    g.xoff[i] = (unsigned)((g.row0 + row) * 1152 + (((lane & 7) ^ ((row >> 1) & 7)) * 16));
    g.woff[i] = (unsigned)row * pitch + (unsigned)(((lane & 7) ^ (4 * ((row >> 1) & 1))) * 16); }
  const int row = 32 * wm + r32;
#pragma unroll
  for (int u = 0; u < 2; ++u) g.xrd[u] = row * 128 + (((2 * u + hi) ^ ((row >> 1) & 7)) * 16);
  const int q = (lane & 15) >> 2, p = lane & 3, blk = (lane >> 4) & 1;
  g.wrd = 8192 + (8 * hi + q) * 128 + (((4 * wn + 2 * blk + (p >> 1)) ^ (4 * ((q >> 1) & 1))) * 16) + (p & 1) * 8;
#pragma unroll
  for (int r = 0; r < 16; ++r) g.acc[r] = 0.f;
}
DI void issue(const Ctx& g, int j, int w4) {
  const unsigned sb = g.ldsb + (unsigned)(j & 1) * 16384u + (unsigned)w4 * 2048u;
  attb::dma16(g.srdX, g.xoff[0], (unsigned)j * 128u, sb); attb::dma16(g.srdX, g.xoff[1], (unsigned)j * 128u, sb + 1024u);
  attb::dma16(g.srdW, g.woff[0], (unsigned)j * g.wstep, sb + 8192u); attb::dma16(g.srdW, g.woff[1], (unsigned)j * g.wstep, sb + 8192u + 1024u);
}
DI void compute(Ctx& g, LAS const char* base  , int j) {
  LAS const char* sb = base + (j & 1) * 16384;
#pragma unroll
  for (int ks = 0; ks < 4; ++ks) {
    const bf16x8 xf = *(const LAS bf16x8*)(sb + (g.xrd[ks & 1] ^ ((ks >> 1) * 64)));
    const bf16x8 wf = cat8(tr_read(sb + g.wrd + ks * 2048), tr_read(sb + g.wrd + ks * 2048 + 512));
    g.acc = MFMA32(wf, xf, g.acc);
  }
}
DI void load_frags(const Ctx& g, LAS const char* sb, bf16x8 (&xf)[4], bf16x8 (&wf)[4]) {
#pragma unroll
  for (int ks = 0; ks < 4; ++ks) {
    xf[ks] = *(const LAS bf16x8*)(sb + (g.xrd[ks & 1] ^ ((ks >> 1) * 64)));
    wf[ks] = cat8(tr_read(sb + g.wrd + ks * 2048), tr_read(sb + g.wrd + ks * 2048 + 512));
  }
}
DI void epilogue(const Ctx& g, int w4, int lane, u16* __restrict__ QA, char* __restrict__ KA, char* __restrict__ VA) {
  const int r32 = lane & 31, hi = lane >> 5, wm = w4 & 1, wn = w4 >> 1, row = g.row0 + 32 * wm + r32;
  if (g.tn < 9) {
#pragma unroll
    for (int rq = 0; rq < 4; ++rq)
      *(u32x2*)(QA + (size_t)row * 576 + g.tn * 64 + 32 * wn + 8 * rq + 4 * hi) = (u32x2){cvtpk(g.acc[4 * rq] * QSCALE_A, g.acc[4 * rq + 1] * QSCALE_A), cvtpk(g.acc[4 * rq + 2] * QSCALE_A, g.acc[4 * rq + 3] * QSCALE_A)};
  } else {
#pragma unroll
    for (int r = 0; r < 16; ++r) {
      const int c = 32 * wn + (r & 3) + 8 * (r >> 2) + 4 * hi; const u16 val = (u16)(cvtpk(g.acc[r], 0.f) & 0xffff);
      if (c < 18) { char* p = c < 8 ? KA + row * 16 + c * 2 : (c < 16 ? KA + 32768 + row * 16 + (c - 8) * 2 : KA + 65536 + row * 4 + (c - 16) * 2); *(u16*)p = val; }
      else if (c <= 36) {
        const int d = c - 18, w16 = row & 15, chunk = 2 * (row >> 4) + ((w16 >> 2) & 1), j = (w16 >> 3) * 4 + (w16 & 3);
        *(u16*)(VA + chunk * 304 + d * 16 + j * 2) = c == 36 ? (u16)0x3F80 : val;
      }
    }
  }
}
struct Iss { __amdgpu_buffer_rsrc_t srdW; unsigned xoff[2], woff[2], wstep; };
DI void iss_init(Iss& g, int id, int w4, int lane, const u16* __restrict__ wq, const u16* __restrict__ wkv) {
  const int row0 = 64 * (id & 31), tn = id >> 5; const bool kv = tn == 9; const unsigned pitch = kv ? 128u : 1152u;
  g.srdW = __builtin_amdgcn_make_buffer_rsrc((void*)(kv ? wkv : wq + tn * 64), (short)0, kv ? 576 * 128 : 576 * 1152 - tn * 128, 0x00020000);
  g.wstep = 64u * pitch;
#pragma unroll
  for (int i = 0; i < 2; ++i) { const int row = 8 * (2 * w4 + i) + (lane >> 3);
    g.xoff[i] = (unsigned)((row0 + row) * 1152 + (((lane & 7) ^ ((row >> 1) & 7)) * 16));
    g.woff[i] = (unsigned)row * pitch + (unsigned)(((lane & 7) ^ (4 * ((row >> 1) & 1))) * 16); }
}
DI void iss(const Iss& g, __amdgpu_buffer_rsrc_t srdX, unsigned stage_addr, int j, int w4) {
  const unsigned sb = stage_addr + (unsigned)w4 * 2048u;
  attb::dma16(srdX, g.xoff[0], (unsigned)j * 128u, sb); attb::dma16(srdX, g.xoff[1], (unsigned)j * 128u, sb + 1024u);
  attb::dma16(g.srdW, g.woff[0], (unsigned)j * g.wstep, sb + 8192u); attb::dma16(g.srdW, g.woff[1], (unsigned)j * g.wstep, sb + 8192u + 1024u);
}
DI void ld1(u32x4& r, __amdgpu_buffer_rsrc_t srd, unsigned voff, unsigned soff) { asm volatile("buffer_load_dwordx4 %0, %1, %2, %3 offen" : "=&v"(r) : "v"(voff), "s"(srd), "s"(soff) : "memory"); }
DI void ld_regs(const Iss& g, __amdgpu_buffer_rsrc_t srdX, int j, u32x4 (&r)[4]) {
  ld1(r[0], srdX, g.xoff[0], (unsigned)j * 128u); ld1(r[1], srdX, g.xoff[1], (unsigned)j * 128u);
  ld1(r[2], g.srdW, g.woff[0], (unsigned)j * g.wstep); ld1(r[3], g.srdW, g.woff[1], (unsigned)j * g.wstep);
}
DI void st_regs(LAS char* sb, int w4, int lane, const u32x4 (&r)[4]) {
  LAS char* p = sb + w4 * 2048 + lane * 16;
  *(LAS u32x4*)p = r[0]; *(LAS u32x4*)(p + 1024) = r[1]; *(LAS u32x4*)(p + 8192) = r[2]; *(LAS u32x4*)(p + 8192 + 1024) = r[3];
}
DI void standalone(LAS char* lds, int id2, const u16* __restrict__ X, const u16* __restrict__ wq, const u16* __restrict__ wkv, u16* __restrict__ QA, char* __restrict__ KA, char* __restrict__ VA) {
  const int tid = threadIdx.x, wid = __builtin_amdgcn_readfirstlane(tid >> 6), lane = tid & 63, w4 = wid & 3, half = wid >> 2;
  Ctx g; init(g, (unsigned)(uintptr_t)lds + (unsigned)half * 32768u, 2 * id2 + half, w4, lane, X, wq, wkv);
  LAS const char* base = lds + half * 32768;
  issue(g, 0, w4); issue(g, 1, w4);
  for (int j = 0; j < 9; ++j) {
    if (j < 8) { WAITV_BAR(4); } else { WAITV_BAR(0); }
    compute(g, base, j);
    asm volatile("s_waitcnt lgkmcnt(0)\n\ts_barrier" ::: "memory");
    if (j + 2 < 9) issue(g, j + 2, w4);
  }
  epilogue(g, w4, lane, QA, KA, VA);
}
}

namespace g1b {
DI void oneshot(LAS char* lds, int id, const u16* __restrict__ X, const u16* __restrict__ wq, const u16* __restrict__ wkv, u16* __restrict__ QA, char* __restrict__ KA, char* __restrict__ VA) {
  const int tid = threadIdx.x, wid = __builtin_amdgcn_readfirstlane(tid >> 6), lane = tid & 63, w4 = wid & 3, kh = wid >> 2;
  const unsigned l0 = (unsigned)(uintptr_t)lds;
  Ctx g; init(g, 0u, id, w4, lane, X, wq, wkv);
  const bool kv = g.tn == 9; const unsigned pitch = kv ? 128u : 1152u;
  const int row = 8 * wid + (lane >> 3);
  const unsigned xo = (unsigned)((g.row0 + row) * 1152 + (((lane & 7) ^ ((row >> 1) & 7)) * 16)), wo = (unsigned)row * pitch + (unsigned)(((lane & 7) ^ (4 * ((row >> 1) & 1))) * 16);
#pragma unroll
  for (int u = 0; u < 9; ++u) { const int kt = (u & 1) ? 5 + (u >> 1) : (u >> 1);
    attb::dma16(g.srdX, xo, (unsigned)kt * 128u, l0 + (unsigned)kt * 16384u + (unsigned)wid * 1024u);
    attb::dma16(g.srdW, wo, (unsigned)kt * g.wstep, l0 + (unsigned)kt * 16384u + 8192u + (unsigned)wid * 1024u); }
  auto comp = [&](int kt) { bf16x8 xf[4], wf[4]; load_frags(g, lds + kt * 16384, xf, wf);
#pragma unroll
    for (int ks = 0; ks < 4; ++ks) g.acc = MFMA32(wf[ks], xf[ks], g.acc); };
  WAITV_BAR(10); comp(kh ? 5 : 0); comp(kh ? 6 : 1);
  WAITV_BAR(2);  comp(kh ? 7 : 2); comp(kh ? 8 : 3);
  WAITV_BAR(0);  if (kh == 0) comp(4);
  asm volatile("s_waitcnt lgkmcnt(0)\n\ts_barrier" ::: "memory");
  LAS char* rb = lds + w4 * 4096 + lane * 16;
  if (kh == 1) {
#pragma unroll
    for (int q = 0; q < 4; ++q) *(LAS f32x4*)(rb + q * 1024) = (f32x4){g.acc[4 * q], g.acc[4 * q + 1], g.acc[4 * q + 2], g.acc[4 * q + 3]};
  }
  asm volatile("s_waitcnt lgkmcnt(0)\n\ts_barrier" ::: "memory");
  if (kh == 0) {
#pragma unroll
    for (int q = 0; q < 4; ++q) { const f32x4 v = *(const LAS f32x4*)(rb + q * 1024);
#pragma unroll
      for (int e = 0; e < 4; ++e) g.acc[4 * q + e] += v[e]; }
    epilogue(g, w4, lane, QA, KA, VA);
  }
}
}
__global__ __launch_bounds__(512, 2) void k2_attn_onlyA(char* __restrict__ ws, const u16* __restrict__ WoB, const u16* __restrict__ WoA) {
  extern __shared__ __attribute__((aligned(16))) char smem[];
  LAS char* lds = (LAS char*)smem;
  const int t = blockIdx.x;
  CLK0();
  atta::run(lds, (const u16*)(ws + WS_QA), ws + WS_KA, ws + WS_OA, (t & 7) + 8 * (t >> 5), (t >> 3) & 3, WoB, WoA, t);
  CLK1(ws, 2);
}
__global__ __launch_bounds__(512, 2) void k2_attn_onlyB(const u16* __restrict__ XB, const u16* __restrict__ WqB, char* __restrict__ ws, const u16* __restrict__ XA, const u16* __restrict__ wq, const u16* __restrict__ wk, const u16* __restrict__ wv) {
  extern __shared__ __attribute__((aligned(16))) char smem[];
  LAS char* lds = (LAS char*)smem;
  const int b = blockIdx.x;
  const int xcd = b & 7, idx = b >> 3;
  CLK0();
  if (b < 256) {
    const int x = idx & 7;
    auto bg = [&](int T0, int NTH, int k3, auto&& ld) {
      using namespace attb;
      __builtin_amdgcn_s_setprio(3);
      for (int t = T0; t < NTH; ++t) {
        const int n = t - T0;
        const int wn = n == 0 ? (T0 + 2 < NTH ? 4 : 0) + 8 : (n == 1 ? 8 + (t - 1 + PD < NTH ? 8 : 0) : (t - 1 + PD < NTH ? 8 : 0));
        wait_bar_n(wn);
        if (t + PD < NTH) {
#pragma unroll
          for (int i = 0; i < 8; ++i) ld(t + PD, k3, i); }
        k3 = k3 == 2 ? 0 : k3 + 1;
      }
    };
    attb::run(lds, XB, WqB, ws + WS_KV, ws + WS_CTX, 4 * xcd + (idx >> 3), x, bg);
  } else {
    const int t = b - 256;
    g1b::oneshot(lds, t < 32 ? 288 + t : t - 32, XA, wq, (const u16*)(ws + WS_WKV), (u16*)(ws + WS_QA), ws + WS_KA, ws + WS_VA);
  }
  CLK1(ws, 1);
}
extern "C" void kernel_launch(void* const* d_in, const int* in_sizes, int n_in, void* d_out, int out_size, void* d_ws, size_t ws_size, hipStream_t stream) {
  const u16 *Ax = (const u16*)d_in[0], *Awq = (const u16*)d_in[1], *Awk = (const u16*)d_in[2], *Awv = (const u16*)d_in[3], *Awo = (const u16*)d_in[4];
  const u16 *Bx = (const u16*)d_in[5], *Bc = (const u16*)d_in[6], *Bwq = (const u16*)d_in[7], *Bwuk = (const u16*)d_in[8], *Bwuv = (const u16*)d_in[9], *Bwo = (const u16*)d_in[10];
  char* ws = (char*)d_ws;
  static bool init = false;
  if (!init) {
    (void)hipFuncSetAttribute((const void*)k1_proj, hipFuncAttributeMaxDynamicSharedMemorySize, g3::STAGE_BYTES);
    (void)hipFuncSetAttribute((const void*)k2_attn_onlyA, hipFuncAttributeMaxDynamicSharedMemorySize, 160 * 1024);
    (void)hipFuncSetAttribute((const void*)k2_attn_onlyB, hipFuncAttributeMaxDynamicSharedMemorySize, 160 * 1024);
    (void)hipFuncSetAttribute((const void*)k3_out, hipFuncAttributeMaxDynamicSharedMemorySize, 96 * 1024);
    init = true; }
  k1_proj<<<256, 512, g3::STAGE_BYTES, stream>>>(Bc, Bwuk, Bwuv, Ax, Awq, Awk, Awv, ws, Bx, Bwq);
  k2_attn_onlyB<<<256 + 320, 512, 160 * 1024, stream>>>(Bx, Bwq, ws, Ax, Awq, Awk, Awv);
  k2_attn_onlyA<<<256, 512, 160 * 1024, stream>>>(ws, Bwo, Awo);
  k3_out<<<256, 512, 96 * 1024, stream>>>(ws, Bwo, Awo, (u16*)d_out);
}
```

```cpp
#include <hip/hip_runtime.h>
#include <stdint.h>
#include <stdio.h>

typedef unsigned short u16;
typedef short bf16x8 __attribute__((ext_vector_type(8)));
typedef short s16x4 __attribute__((ext_vector_type(4)));
typedef float f32x4 __attribute__((ext_vector_type(4)));
typedef float f32x2 __attribute__((ext_vector_type(2)));
typedef float f32x16 __attribute__((ext_vector_type(16)));
typedef unsigned u32x4 __attribute__((ext_vector_type(4)));
typedef unsigned u32x2 __attribute__((ext_vector_type(2)));
typedef unsigned u32x32 __attribute__((ext_vector_type(32)));
typedef __bf16 bf16x2_t __attribute__((ext_vector_type(2)));
#define LAS __attribute__((address_space(3)))
#define DI __device__ __forceinline__
#define SBAR() __builtin_amdgcn_sched_barrier(0)
#define LGKM0() __builtin_amdgcn_s_waitcnt(0xC07F)
#define PIN(x) asm volatile("" : "+v"(x))

DI unsigned cvtpk(float lo, float hi) { f32x2 v = {lo, hi}; bf16x2_t b = __builtin_convertvector(v, bf16x2_t); return __builtin_bit_cast(unsigned, b); }
DI __amdgpu_buffer_rsrc_t mk_srd(const void* p, unsigned bytes) { return __builtin_amdgcn_make_buffer_rsrc((void*)p, (short)0, (int)bytes, 0x00020000); }
DI __amdgpu_buffer_rsrc_t mk_srd_u(const void* p, unsigned bytes) {
  const unsigned long long pa = (unsigned long long)p;
  const unsigned long long pu = ((unsigned long long)(unsigned)__builtin_amdgcn_readfirstlane((unsigned)(pa >> 32)) << 32) | (unsigned)__builtin_amdgcn_readfirstlane((unsigned)pa);
  return __builtin_amdgcn_make_buffer_rsrc((void*)pu, (short)0, __builtin_amdgcn_readfirstlane(bytes), 0x00020000);
}
DI float bf2f(u16 b) { return __uint_as_float(((unsigned)b) << 16); }
DI s16x4 tr_read(LAS const char* p) { typedef short v4i16_t __attribute__((ext_vector_type(4))); return __builtin_bit_cast(s16x4, __builtin_amdgcn_ds_read_tr16_b64_v4i16((LAS v4i16_t*)p)); }
DI bf16x8 cat8(s16x4 a, s16x4 b) { return __builtin_shufflevector(a, b, 0, 1, 2, 3, 4, 5, 6, 7); }

constexpr int S = 2048;
#define CTX_PAD 0
constexpr int CTX_RB = 262144 + CTX_PAD;
constexpr size_t WS_KV   = 0;
constexpr size_t WS_CTX  = WS_KV + (size_t)32 * 32 * 32768;
constexpr size_t WS_QA   = WS_CTX + (size_t)64 * CTX_RB;
constexpr size_t WS_KA   = WS_QA + (size_t)S * 576 * 2;
constexpr size_t WS_VA   = WS_KA + 73728;
constexpr size_t WS_OA   = WS_VA + 77824;
constexpr size_t WS_CNT = WS_OA + (size_t)S * 576 * 2;
constexpr size_t WS_SYNC = WS_CNT;
constexpr size_t WS_WKV = WS_CNT + 65536;
constexpr size_t WS_PART = WS_WKV + 576 * 128;
constexpr float QSCALE_A = 0.23570226039551584f * 1.4426950408889634f;
constexpr float QSCALE_B = 0.08838834764831845f * 1.4426950408889634f;

#define CLK0() do {} while (0)
#define CLK1(wsp, k) do {} while (0)

#define ST(wsp, k, i) do {} while (0)

typedef __attribute__((address_space(1))) unsigned gu32;
#define GRLX __ATOMIC_RELAXED, __HIP_MEMORY_SCOPE_AGENT
constexpr int SY_KV = 0;
constexpr int SY_G1 = 256;
constexpr int SY_CTX = 640;
constexpr int SY_OA = 1152;
constexpr int SY_PART = 1408;
constexpr int SY_EPOCH = 1728;
constexpr int SY_WORDS = 1664 + 128;
DI void sy_set(gu32* w, unsigned tag) { __hip_atomic_store(w, tag, GRLX); }
template <int NL> DI void sy_wait(gu32* w, int n, unsigned tag, int lane) {
  for (unsigned spins = 0; spins < (1u << 20); ++spins) {
    bool ok = true;
#pragma unroll
    for (int i = 0; i < NL; ++i) { const int j = lane + 64 * i; if (j < n) ok &= (__hip_atomic_load(w + j, GRLX) == tag); }
    if (__all(ok)) break;
    __builtin_amdgcn_s_sleep(8);
  }
}
DI void sy_acquire() { __builtin_amdgcn_fence(__ATOMIC_ACQUIRE, "agent"); asm volatile("s_waitcnt vmcnt(0)" ::: "memory"); }
DI void st16(__amdgpu_buffer_rsrc_t srd, unsigned off, u32x4 v) { __builtin_amdgcn_raw_buffer_store_b128(v, srd, off, 0, 16); }
DI void sy_release() {}
DI void st8g(void* p, u32x2 v) { __hip_atomic_store((__attribute__((address_space(1))) unsigned long long*)p, ((unsigned long long)v[1] << 32) | v[0], GRLX); }
DI void st4g(void* p, unsigned v) { __hip_atomic_store((gu32*)p, v, GRLX); }
DI void st2g(void* p, u16 v) { __hip_atomic_store((__attribute__((address_space(1))) u16*)p, v, GRLX); }
#include <utility>
namespace attb {
#define ATTB_D 4
constexpr int PD = 3;
constexpr int D = ATTB_D;
constexpr float MARGIN = 48.f;
#define MFMA32(a, b, c) __builtin_amdgcn_mfma_f32_32x32x16_bf16((a), (b), (c), 0, 0, 0)
#define MFMA16(a, b, c) __builtin_amdgcn_mfma_f32_16x16x32_bf16((a), (b), (c), 0, 0, 0)
#define EXP2(x) __builtin_amdgcn_exp2f(x)
template <int... Is, class F> DI void sfor_(std::integer_sequence<int, Is...>, F&& f) { (f(std::integral_constant<int, Is>{}), ...); }
template <int N, class F> DI void sfor(F&& f) { sfor_(std::make_integer_sequence<int, N>{}, f); }
DI void dma16(__amdgpu_buffer_rsrc_t srd, unsigned voff, unsigned soff, unsigned ldsdst) {
  asm volatile("s_mov_b32 m0, %0\n\ts_nop 0\n\tbuffer_load_dwordx4 %1, %2, %3 offen lds" :: "s"(__builtin_amdgcn_readfirstlane(ldsdst)), "v"(voff), "s"(srd), "s"(__builtin_amdgcn_readfirstlane(soff)) : "memory");
}
#define WAITV_BAR(N) asm volatile("s_waitcnt vmcnt(" #N ") lgkmcnt(0)\n\ts_barrier" ::: "memory")
#define WAITV_BAR_NL(N) asm volatile("s_waitcnt vmcnt(" #N ")\n\ts_barrier" ::: "memory")
DI void wait_bar_n(int n) {
  switch (n >> 2) {
    case 0: WAITV_BAR(0); break; case 1: WAITV_BAR(4); break; case 2: WAITV_BAR(8); break; case 3: WAITV_BAR(12); break;
    case 4: WAITV_BAR(16); break; case 5: WAITV_BAR(20); break; case 6: WAITV_BAR(24); break; default: WAITV_BAR(28); break; }
}
DI unsigned ppk(float lo, float hi) { return cvtpk(lo, hi); }
#define LMFMA(P) do { c.L = MFMA16(c.SEL, PF(P), c.L); } while (0)
#define RSUM(a, b) do {} while (0)
template <int N> DI void waitv_bar_c() { asm volatile("s_waitcnt vmcnt(%0) lgkmcnt(0)\n\ts_barrier" :: "i"(N) : "memory"); }
DI void wait_bar_any(int n) {
  switch (n) {
    case 0: waitv_bar_c<0>(); break; case 6: waitv_bar_c<6>(); break; case 7: waitv_bar_c<7>(); break; case 8: waitv_bar_c<8>(); break; case 12: waitv_bar_c<12>(); break;
    case 14: waitv_bar_c<14>(); break; case 15: waitv_bar_c<15>(); break; case 16: waitv_bar_c<16>(); break; default: waitv_bar_c<0>(); break; }
}
#define ATTB_HP 0
constexpr int HP = ATTB_HP;
#define PF(p) __builtin_bit_cast(bf16x8, p)
#define CVT4(P, Sx, b) do { P[0] = ppk(Sx[(b)], Sx[(b) + 1]); P[1] = ppk(Sx[(b) + 2], Sx[(b) + 3]); P[2] = ppk(Sx[(b) + 4], Sx[(b) + 5]); P[3] = ppk(Sx[(b) + 6], Sx[(b) + 7]); } while (0)
#define EXP8(Sx, b) do { _Pragma("unroll") for (int e_ = 0; e_ < 8; ++e_) Sx[(b) + e_] = EXP2(Sx[(b) + e_]); } while (0)

DI void mask16(f32x16& s, int dq) {
#pragma unroll
  for (int r = 0; r < 16; ++r) { const int c = (r & 3) + 8 * (r >> 2); s[r] = (c <= dq) ? s[r] : -__builtin_inff(); }
}

struct St {
  f32x16 O[4], S0, S1, NEGM;
  u32x4 P0, P1, P2, P3;
  f32x4 L;
  float l0, l1;
  bf16x8 Q[8], SEL, Fc[D];
};

#define STB(i) do {} while (0)
template <class BG, class KW, class FL, class PS>
DI void run(LAS char* lds, const u16* __restrict__ X, const u16* __restrict__ Wq, const char* __restrict__ kvimg, char* __restrict__ ctx, int head, int x, BG&& bg, KW&& kvwait, FL&& flag, PS&& post) {
  const int tid = threadIdx.x, wid = __builtin_amdgcn_readfirstlane(tid >> 6), lane = tid & 63, r32 = lane & 31, hi = lane >> 5;
  const unsigned lds0 = (unsigned)(uintptr_t)lds;
  const int grp = wid >> 2, wq = wid & 3;
  const int blk = grp == 0 ? 15 - x : x;
  const int row0 = blk * 128 + wq * 32;
  const int NTH = 32 - 2 * x;
  const int NTW = 2 * blk + 2;
  St c;
  STB(0);
  const __amdgpu_buffer_rsrc_t srdKV = mk_srd((kvimg + (size_t)head * 32 * 32768), 32 * 32768);
  const unsigned kvoff = (unsigned)(lane * 16);
  auto kv_piece = [&](int t, int k3, int p) { dma16(srdKV, kvoff, (unsigned)t * 32768u + (unsigned)p * 1024u, lds0 + (unsigned)(p < 16 ? k3 : (t & 3)) * 32768u + (unsigned)p * 1024u); };
  auto stage_piece_all = [&](int t, int k3, int i) { kv_piece(t, k3, wid * 4 + i); };
  auto stage_piece_ld = [&](int t, int k3, int i) { kv_piece(t, k3, wq * (8 - HP) + i); };
  auto stage_piece_hv = [&](int t, int k3, int i) { kv_piece(t, k3, 32 - 4 * HP + wq * HP + i); };
  {
    f32x16 qa[4];
#pragma unroll
    for (int i = 0; i < 4; ++i)
#pragma unroll
      for (int r = 0; r < 16; ++r) qa[i][r] = 0.f;
    const __amdgpu_buffer_rsrc_t srdX = mk_srd(X, S * 576 * 2);
    const __amdgpu_buffer_rsrc_t srdW = mk_srd((Wq + head * 128), 576 * 4096 * 2 - head * 256);
    unsigned xoff[4];
#pragma unroll
    for (int i = 0; i < 4; ++i) { const int rl = 8 * i + (lane >> 3); xoff[i] = (unsigned)((row0 + rl) * 1152 + (((lane & 7) ^ ((rl >> 1) & 7)) * 16)); }
    unsigned woff[2];
#pragma unroll
    for (int i = 0; i < 2; ++i) woff[i] = (unsigned)((wid * 8 + 4 * i + (lane >> 4)) * 8192 + (((lane & 15) ^ (4 * (lane >> 4))) * 16));
    auto stage = [&](int kt) {
      const unsigned sb = lds0 + (unsigned)(kt % 3) * 49152u;
#pragma unroll
      for (int i = 0; i < 4; ++i) dma16(srdX, xoff[i], (unsigned)kt * 128u, sb + (unsigned)wid * 4096u + i * 1024u);
#pragma unroll
      for (int i = 0; i < 2; ++i) dma16(srdW, woff[i], (unsigned)kt * 64u * 8192u, sb + 32768u + (unsigned)wid * 2048u + i * 1024u);
    };
    const int xrd = wid * 4096 + r32 * 128;
    const int xsw = (r32 >> 1) & 7;
    int wrd[4];
#pragma unroll
    for (int cb = 0; cb < 4; ++cb) { const int tq = (lane & 15) >> 2, tp = lane & 3; wrd[cb] = 32768 + (8 * hi + tq) * 256 + ((4 * (cb ^ tq) + 2 * ((lane >> 4) & 1) + (tp >> 1)) * 16) + (tp & 1) * 8; }
    auto piece = [&](int kt, int i) {
      const unsigned sb = lds0 + (unsigned)(kt % 3) * 49152u;
      if (i < 4) dma16(srdX, xoff[i], (unsigned)kt * 128u, sb + (unsigned)wid * 4096u + i * 1024u);
      else dma16(srdW, woff[i - 4], (unsigned)kt * 64u * 8192u, sb + 32768u + (unsigned)wid * 2048u + (i - 4) * 1024u);
    };
    auto rdw = [&](LAS const char* sb, int i) { return cat8(tr_read(sb + wrd[i & 3] + (i >> 2) * 4096), tr_read(sb + wrd[i & 3] + (i >> 2) * 4096 + 1024)); };
    auto rdx = [&](LAS const char* sb, int ks) { return *(const LAS bf16x8*)(sb + xrd + (((2 * ks + hi) ^ xsw) * 16)); };
    stage(0); stage(1); stage(2);
    post();
    LGKM0();
    WAITV_BAR(14);
    bf16x8 wc[4], xc;
#pragma unroll
    for (int i = 0; i < 4; ++i) wc[i] = rdw(lds, i);
    xc = rdx(lds, 0);
    auto qtile = [&](auto KTC) { constexpr int kt = KTC;
      LAS const char* sb = lds + (kt % 3) * 49152; LAS const char* sn = lds + ((kt + 1) % 3) * 49152;
      bf16x8 wf[20], xf[5];
#pragma unroll
      for (int i = 0; i < 4; ++i) wf[i] = wc[i];
      xf[0] = xc;
      sfor<16>([&](auto I) { constexpr int i = I;
        if constexpr (i == 12 && kt == 6) kvwait();
        if constexpr (i == 12 && kt < 8) { if constexpr (kt < 2) { WAITV_BAR(8); } else if constexpr (kt < 7) { WAITV_BAR(6); } else { WAITV_BAR(4); } SBAR(); }
        if constexpr (i == 12 && kt == 2) flag();
        qa[i & 3] = MFMA32(wf[i], xf[i >> 2], qa[i & 3]); PIN(qa[i & 3]);
        if constexpr (i + 4 < 16) wf[i + 4] = rdw(sb, i + 4); else if constexpr (kt < 8) wf[i + 4] = rdw(sn, i + 4 - 16);
        if constexpr ((i & 3) == 0 && i < 12) xf[(i >> 2) + 1] = rdx(sb, (i >> 2) + 1);
        if constexpr (i == 13 && kt < 8) xf[4] = rdx(sn, 0);
        if constexpr (kt < 6) { if constexpr (i >= 12) piece(kt + 3, i - 12); }
        if constexpr (kt >= 1 && kt <= 6 && i < 2) piece(kt + 2, 4 + i);
        if constexpr (kt == 6 && i >= 12) stage_piece_all(0, 0, i - 12);
        if constexpr (kt == 7 && i >= 12) { stage_piece_all(1, 1, i - 12); stage_piece_all(2, 2, i - 12); }
        SBAR(); });
      if constexpr (kt < 8) {
#pragma unroll
        for (int i = 0; i < 4; ++i) wc[i] = wf[16 + i];
        xc = xf[4]; }
    };
    sfor<9>(qtile);
#pragma unroll
    for (int cb = 0; cb < 4; ++cb)
#pragma unroll
      for (int u = 0; u < 2; ++u) {
        u32x4 w;
#pragma unroll
        for (int e = 0; e < 4; ++e) w[e] = cvtpk(qa[cb][8 * u + 2 * e] * QSCALE_B, qa[cb][8 * u + 2 * e + 1] * QSCALE_B);
        c.Q[2 * cb + u] = __builtin_bit_cast(bf16x8, w);
      }
  }
  STB(1);
  LAS const char* kb_l = lds + hi * 1024 + r32 * 16;
  LAS const char* vb_l = lds + 16384 + hi * 2048 + r32 * 16;
#pragma unroll
  for (int i = 0; i < 4; ++i)
#pragma unroll
    for (int r = 0; r < 16; ++r) c.O[i][r] = 0.f;
  c.L = (f32x4){0.f, 0.f, 0.f, 0.f}; c.l0 = 0.f; c.l1 = 0.f;
  { const short one = ((((lane >> 4) & 1) == (((lane & 15) >> 2) & 1)) ? (short)0x3F80 : (short)0);
#pragma unroll
    for (int j = 0; j < 8; ++j) c.SEL[j] = one; }
  const int qrow = row0 + r32 - 4 * hi;
#define KF(base, kb, s) (*(const LAS bf16x8*)((base) + (s) * 2048 + (kb) * 512))
#define VF(base, ks, db) (*(const LAS bf16x8*)((base) + (ks) * 4096 + (db) * 512))
  WAITV_BAR(8);
  {
    f32x16 z;
#pragma unroll
    for (int r = 0; r < 16; ++r) z[r] = 0.f;
    c.S0 = z; c.S1 = z;
#pragma unroll
    for (int s = 0; s < 8; ++s) c.S0 = MFMA32(KF(kb_l, 0, s), c.Q[s], c.S0);
#pragma unroll
    for (int s = 0; s < 8; ++s) c.S1 = MFMA32(KF(kb_l, 1, s), c.Q[s], c.S1);
    if (NTW == 2) { mask16(c.S0, qrow); mask16(c.S1, qrow - 32); }
    float m = c.S0[0];
#pragma unroll
    for (int r = 1; r < 16; ++r) m = __builtin_fmaxf(m, c.S0[r]);
#pragma unroll
    for (int r = 0; r < 16; ++r) m = __builtin_fmaxf(m, c.S1[r]);
    { auto rr = __builtin_amdgcn_permlane32_swap(__float_as_uint(m), __float_as_uint(m), false, false); m = __builtin_fmaxf(__uint_as_float(rr[0]), __uint_as_float(rr[1])); }
    const float nm = -(m + MARGIN);
#pragma unroll
    for (int r = 0; r < 16; ++r) { c.NEGM[r] = nm; c.S0[r] += nm; c.S1[r] += nm; }
    EXP8(c.S0, 0); EXP8(c.S0, 8); CVT4(c.P0, c.S0, 0); CVT4(c.P1, c.S0, 8);
    EXP8(c.S1, 0); CVT4(c.P2, c.S1, 0);
#pragma unroll
    for (int e = 0; e < 16; e += 2) RSUM(c.S0[e], c.S0[e + 1]);
#pragma unroll
    for (int e = 0; e < 6; e += 2) RSUM(c.S1[e], c.S1[e + 1]);
  }
  WAITV_BAR(4);
#pragma unroll
  for (int i = 0; i < 4; ++i) stage_piece_all(3, 0, i);
  {
#pragma unroll
    for (int db = 0; db < 4; ++db) c.O[db] = MFMA32(VF(vb_l, 0, db), PF(c.P0), c.O[db]);
    LMFMA(c.P0);
#pragma unroll
    for (int db = 0; db < 4; ++db) c.O[db] = MFMA32(VF(vb_l, 1, db), PF(c.P1), c.O[db]);
    LMFMA(c.P1);
#pragma unroll
    for (int i = 0; i < D; ++i) c.Fc[i] = KF(kb_l + 32768, 0, i);
  }
  const int T0 = 2 * x + 2;
  auto step = [&](auto GENC, auto DMAC, int t, int k3) {
    constexpr bool GEN = GENC, DMA = DMAC;
    LAS const char* vk = kb_l + k3 * 32768;
    LAS const char* vv = vb_l + (t & 3) * 32768;
    LAS const char* vvp = vb_l + ((t - 1) & 3) * 32768;
    LAS const char* vkn = kb_l + (k3 == 2 ? 0 : k3 + 1) * 32768;
    const bool st3 = GEN ? (t + PD < NTH && t < T0) : DMA, msk = GEN && t >= NTW - 2;
    int wn = 0;
    if (GEN) {
#pragma unroll
      for (int u = 2; u < PD; ++u) wn += (t + u < NTH && t + u <= T0 + PD - 1) ? 4 : 0; }
    bf16x8 F[32 + D];
#pragma unroll
    for (int i = 0; i < D; ++i) F[i] = c.Fc[i];
    SBAR();
    sfor<32>([&](auto I) { constexpr int i = I; constexpr int j = i + D;
      if constexpr (i == 24) {
        if constexpr (GEN) wait_bar_n(wn); else if constexpr (DMA) { if constexpr (PD == 3) { WAITV_BAR_NL(4); } else { WAITV_BAR_NL(8); } } else { asm volatile("s_waitcnt vmcnt(%0)\n\ts_barrier" :: "i"(HP) : "memory"); }
        SBAR();
      }
      constexpr int ph = i >> 3, u = i & 7;
      if constexpr (ph == 0) { c.S0 = MFMA32(F[i], c.Q[u], u == 0 ? c.NEGM : c.S0); PIN(c.S0); }
      else if constexpr (ph == 1) { c.O[u & 3] = MFMA32(F[i], u < 4 ? PF(c.P2) : PF(c.P3), c.O[u & 3]); PIN(c.O[u & 3]);
        if constexpr (u == 3) { LMFMA(c.P2); PIN(c.L); }
        if constexpr (u == 7) { LMFMA(c.P3); PIN(c.L); } }
      else if constexpr (ph == 2) { c.S1 = MFMA32(F[i], c.Q[u], u == 0 ? c.NEGM : c.S1); PIN(c.S1); }
      else { c.O[u & 3] = MFMA32(F[i], u < 4 ? PF(c.P0) : PF(c.P1), c.O[u & 3]); PIN(c.O[u & 3]);
        if constexpr (u == 3) { LMFMA(c.P0); PIN(c.L); }
        if constexpr (u == 7) { LMFMA(c.P1); PIN(c.L); } }
      if constexpr (j < 8) F[j] = KF(vk, 0, j);
      else if constexpr (j < 16) F[j] = VF(vvp, 2 + ((j - 8) >> 2), (j - 8) & 3);
      else if constexpr (j < 24) F[j] = KF(vk, 1, j - 16);
      else if constexpr (j < 32) F[j] = VF(vv, (j - 24) >> 2, (j - 24) & 3);
      else F[j] = KF(vkn, 0, j - 32);
      auto ex = [&](f32x16& X, int e) { X[e] = EXP2(X[e]); };
      if constexpr (ph == 0) {
        if constexpr (u == 0) { c.P2[3] = ppk(c.S1[6], c.S1[7]); RSUM(c.S1[6], c.S1[7]); PIN(c.P2); }
        if constexpr (u >= 2 && !(u & 1)) { c.P3[(u - 2) >> 1] = ppk(c.S1[8 + u - 2], c.S1[8 + u - 1]); RSUM(c.S1[8 + u - 2], c.S1[8 + u - 1]); PIN(c.P3); }
        ex(c.S1, 8 + u); PIN(c.S1);
      } else if constexpr (ph == 2) {
        if constexpr (u == 0) { c.P0[3] = ppk(c.S0[6], c.S0[7]); RSUM(c.S0[6], c.S0[7]); PIN(c.P0); }
        if constexpr (u >= 2 && !(u & 1)) { c.P1[(u - 2) >> 1] = ppk(c.S0[8 + u - 2], c.S0[8 + u - 1]); RSUM(c.S0[8 + u - 2], c.S0[8 + u - 1]); PIN(c.P1); }
        ex(c.S0, 8 + u); PIN(c.S0);
      } else if constexpr (ph == 1) {
        if constexpr (u == 0) { c.P3[3] = ppk(c.S1[14], c.S1[15]); RSUM(c.S1[14], c.S1[15]); PIN(c.P3); }
        if constexpr (GEN && u == 1) { if (msk) mask16(c.S0, qrow - t * 64); PIN(c.S0); }
        if constexpr (u == 3) { c.P0[0] = ppk(c.S0[0], c.S0[1]); RSUM(c.S0[0], c.S0[1]); PIN(c.P0); }
        if constexpr (u == 4) { c.P0[1] = ppk(c.S0[2], c.S0[3]); RSUM(c.S0[2], c.S0[3]); PIN(c.P0); }
        if constexpr (u == 6) { c.P0[2] = ppk(c.S0[4], c.S0[5]); RSUM(c.S0[4], c.S0[5]); PIN(c.P0); }
        if constexpr (u == 2) { ex(c.S0, 0); ex(c.S0, 1); PIN(c.S0); }
        if constexpr (u == 3) { ex(c.S0, 2); ex(c.S0, 3); PIN(c.S0); }
        if constexpr (u >= 4) { ex(c.S0, u); PIN(c.S0); }
      } else {
        if constexpr (u == 0) { c.P1[3] = ppk(c.S0[14], c.S0[15]); RSUM(c.S0[14], c.S0[15]); PIN(c.P1); }
        if constexpr (!(u & 1)) { if (st3) stage_piece_all(t + 3, k3, u >> 1); }
        if constexpr (!GEN && !DMA && (u == 1 || u == 5) && (u >> 2) < HP) { if (t + 3 < NTH) stage_piece_hv(t + 3, k3, u >> 2); }
        if constexpr (GEN && u == 1) { if (msk) mask16(c.S1, qrow - t * 64 - 32); PIN(c.S1); }
        if constexpr (u == 3) { c.P2[0] = ppk(c.S1[0], c.S1[1]); RSUM(c.S1[0], c.S1[1]); PIN(c.P2); }
        if constexpr (u == 4) { c.P2[1] = ppk(c.S1[2], c.S1[3]); RSUM(c.S1[2], c.S1[3]); PIN(c.P2); }
        if constexpr (u == 6) { c.P2[2] = ppk(c.S1[4], c.S1[5]); RSUM(c.S1[4], c.S1[5]); PIN(c.P2); }
        if constexpr (u == 2) { ex(c.S1, 0); ex(c.S1, 1); PIN(c.S1); }
        if constexpr (u == 3) { ex(c.S1, 2); ex(c.S1, 3); PIN(c.S1); }
        if constexpr (u >= 4) { ex(c.S1, u); PIN(c.S1); }
      }
      SBAR();
    });
#pragma unroll
    for (int i = 0; i < D; ++i) c.Fc[i] = F[32 + i];
  };
  STB(2);
  int t = 1, k3 = 1;
#define NXT() do { ++t; k3 = k3 == 2 ? 0 : k3 + 1; } while (0)
  { const int e1 = (NTW - 2 < T0) ? NTW - 2 : T0;
    const int e1b = e1 < NTH - PD ? e1 : NTH - PD;
    LGKM0();
    while (t < e1b) { step(std::false_type{}, std::true_type{}, t, k3); NXT(); }
    while (t < T0 && t < NTW) { step(std::true_type{}, std::true_type{}, t, k3); NXT(); }
    LGKM0();
    while (t < NTW - 2) { step(std::false_type{}, std::false_type{}, t, k3); NXT(); } }
  while (t < NTW) { step(std::true_type{}, std::false_type{}, t, k3); NXT(); }
  STB(3);
  {
    LAS const char* vvp = vb_l + ((NTW - 1) & 3) * 32768;
    c.P2[3] = ppk(c.S1[6], c.S1[7]); RSUM(c.S1[6], c.S1[7]);
    EXP8(c.S1, 8); CVT4(c.P3, c.S1, 8);
#pragma unroll
    for (int e = 8; e < 16; e += 2) RSUM(c.S1[e], c.S1[e + 1]);
#pragma unroll
    for (int db = 0; db < 4; ++db) c.O[db] = MFMA32(VF(vvp, 2, db), PF(c.P2), c.O[db]);
    LMFMA(c.P2);
#pragma unroll
    for (int db = 0; db < 4; ++db) c.O[db] = MFMA32(VF(vvp, 3, db), PF(c.P3), c.O[db]);
    LMFMA(c.P3);
  }
  {
    float chk = c.L[0];
#pragma unroll
    for (int db = 0; db < 4; ++db)
#pragma unroll
      for (int r = 0; r < 16; ++r) chk = __builtin_fmaf(c.O[db][r], 0.f, chk);
    const bool bad = !(chk < 1.0e37f) || !(c.L[0] > 0.f);
    if (__builtin_expect(__any(bad), 0)) {
      const char* kvh = kvimg + (size_t)head * 32 * 32768;
      float m = -1.0e30f, l = 0.f;
#pragma unroll
      for (int i = 0; i < 4; ++i)
#pragma unroll
        for (int r = 0; r < 16; ++r) c.O[i][r] = 0.f;
#pragma unroll 1
      for (int t = 0; t < NTW; ++t) {
        const char* kt = kvh + (size_t)t * 32768 + r32 * 16;
#pragma unroll
        for (int r = 0; r < 16; ++r) { c.S0[r] = 0.f; c.S1[r] = 0.f; }
#pragma unroll
        for (int s = 0; s < 8; ++s) {
          c.S0 = MFMA32(*(const bf16x8*)(kt + (2 * s + hi) * 1024), c.Q[s], c.S0);
          c.S1 = MFMA32(*(const bf16x8*)(kt + (2 * s + hi) * 1024 + 512), c.Q[s], c.S1); }
        if (t >= NTW - 2) { mask16(c.S0, qrow - t * 64); mask16(c.S1, qrow - t * 64 - 32); }
        float mx = c.S0[0];
#pragma unroll
        for (int r = 1; r < 16; ++r) mx = __builtin_fmaxf(mx, c.S0[r]);
#pragma unroll
        for (int r = 0; r < 16; ++r) mx = __builtin_fmaxf(mx, c.S1[r]);
        { auto rr = __builtin_amdgcn_permlane32_swap(__float_as_uint(mx), __float_as_uint(mx), false, false); mx = __builtin_fmaxf(__uint_as_float(rr[0]), __uint_as_float(rr[1])); }
        const float mn = __builtin_fmaxf(m, mx), alpha = EXP2(m - mn);
        m = mn; l *= alpha;
#pragma unroll
        for (int r = 0; r < 16; ++r) { c.S0[r] = EXP2(c.S0[r] - mn); c.S1[r] = EXP2(c.S1[r] - mn); l += c.S0[r] + c.S1[r]; }
#pragma unroll
        for (int i = 0; i < 4; ++i)
#pragma unroll
          for (int r = 0; r < 16; ++r) c.O[i][r] *= alpha;
        c.P0 = (u32x4){cvtpk(c.S0[0], c.S0[1]), cvtpk(c.S0[2], c.S0[3]), cvtpk(c.S0[4], c.S0[5]), cvtpk(c.S0[6], c.S0[7])};
        c.P1 = (u32x4){cvtpk(c.S0[8], c.S0[9]), cvtpk(c.S0[10], c.S0[11]), cvtpk(c.S0[12], c.S0[13]), cvtpk(c.S0[14], c.S0[15])};
        c.P2 = (u32x4){cvtpk(c.S1[0], c.S1[1]), cvtpk(c.S1[2], c.S1[3]), cvtpk(c.S1[4], c.S1[5]), cvtpk(c.S1[6], c.S1[7])};
        c.P3 = (u32x4){cvtpk(c.S1[8], c.S1[9]), cvtpk(c.S1[10], c.S1[11]), cvtpk(c.S1[12], c.S1[13]), cvtpk(c.S1[14], c.S1[15])};
#pragma unroll
        for (int db = 0; db < 4; ++db) {
          const char* vt = kt + 16384 + hi * 2048 + db * 512;
          c.O[db] = MFMA32(*(const bf16x8*)(vt), PF(c.P0), c.O[db]); c.O[db] = MFMA32(*(const bf16x8*)(vt + 4096), PF(c.P1), c.O[db]);
          c.O[db] = MFMA32(*(const bf16x8*)(vt + 8192), PF(c.P2), c.O[db]); c.O[db] = MFMA32(*(const bf16x8*)(vt + 12288), PF(c.P3), c.O[db]); }
      }
      { auto rr = __builtin_amdgcn_permlane32_swap(__float_as_uint(l), __float_as_uint(l), false, false); l = __uint_as_float(rr[0]) + __uint_as_float(rr[1]); }
      c.L[0] = l;
    }
  }
  {
    const float linv = 1.0f / c.L[0];
    const __amdgpu_buffer_rsrc_t srdC = mk_srd((ctx + (size_t)(row0 >> 5) * CTX_RB), CTX_RB);
    const unsigned cbase = (unsigned)((head * 16 + hi) * 512 + r32 * 16);
#pragma unroll
    for (int db = 0; db < 4; ++db)
#pragma unroll
      for (int i = 0; i < 2; ++i) {
        u32x4 w;
#pragma unroll
        for (int e = 0; e < 4; ++e) w[e] = cvtpk(c.O[db][8 * i + 2 * e] * linv, c.O[db][8 * i + 2 * e + 1] * linv);
        st16(srdC, cbase + (db * 4 + i * 2) * 512, w);
      }
  }
  STB(4);
  if (grp == 1) bg(T0, NTH, k3, [&](int tt, int kk3, int i) { stage_piece_ld(tt, kk3, i); });
}
}
namespace g3 {
constexpr int BK = 64, HALF = 128, HTB = HALF * BK * 2, STAGE_BYTES = 8 * HTB, KD = 512, NT = KD / BK;
DI int lds_byte(int r, int c) { const int st = (r >> 4) * 2 + (c >> 5), rr = r & 15, cc = c & 31, ob = rr * 64 + cc * 2; return st * 1024 + (ob ^ (((ob >> 9) & 1) << 5)); }
DI void stage_rc(int b, int& R, int& C) { const int st = b / 1024, sb = b % 1024, swz = sb ^ (((sb >> 9) & 1) << 5); R = (st >> 1) * 16 + swz / 64; C = (st & 1) * 32 + (swz % 64) / 2; }

DI void kv_first(LAS char* lds, const u16* __restrict__ Ckv, const u16* __restrict__ W, int pm, int pnl) {
  const int tid = threadIdx.x, wid = __builtin_amdgcn_readfirstlane(tid >> 6);
  const unsigned lds0 = (unsigned)(uintptr_t)lds + (unsigned)wid * 1024u;
  const __amdgpu_buffer_rsrc_t sA = mk_srd(Ckv, 2048 * KD * 2), sB = mk_srd(W, KD * 4096 * 2);
  unsigned vA[2], vB[2];
#pragma unroll
  for (int i = 0; i < 2; ++i) {
    int R, C; stage_rc(tid * 16 + i * 8192, R, C); vA[i] = (unsigned)(R * KD + C) * 2u;
    const int k = (tid >> 4) + 32 * i, n = ((tid & 15) ^ (2 * (k & 3) + 8 * ((k >> 3) & 1))) * 8;
    vB[i] = (unsigned)(k * 4096 + n) * 2u; }
  const unsigned oa = (unsigned)(pm * 256 * KD * 2), ob = (unsigned)(pnl * 512);
#pragma unroll
  for (int h = 0; h < 2; ++h)
#pragma unroll
    for (int i = 0; i < 2; ++i) attb::dma16(sB, vB[i], ob + h * HALF * 2, lds0 + (4 + h) * HTB + i * 8192u);
#pragma unroll
  for (int h = 0; h < 2; ++h)
#pragma unroll
    for (int i = 0; i < 2; ++i) attb::dma16(sA, vA[i], oa + h * (HALF * KD * 2), lds0 + h * HTB + i * 8192u);
}
template <bool ISK, class FL, class AL>
DI void kv_tile(LAS char* lds, const u16* __restrict__ Ckv, const u16* __restrict__ W, int pm, int pnl, u32x32& pkA, u32x32& pkB, unsigned& lanebase, FL&& flag, AL&& after_loop) {
  u32x4 pk[16];
  const int tid = threadIdx.x, wid = __builtin_amdgcn_readfirstlane(tid >> 6), lane = tid & 63, wr = wid >> 2, wc = wid & 3, fr = lane & 15, fq = lane >> 4;
  unsigned voffA[2], voffB[2];
#pragma unroll
  for (int i = 0; i < 2; ++i) {
    int R, C; stage_rc(tid * 16 + i * 8192, R, C); voffA[i] = (unsigned)(R * KD + C) * 2u;
    const int k = (tid >> 4) + 32 * i, n = ((tid & 15) ^ (2 * (k & 3) + 8 * ((k >> 3) & 1))) * 8;
    voffB[i] = (unsigned)(k * 4096 + n) * 2u;
  }
  constexpr size_t kstepA = BK * 2, kstepB = (size_t)BK * 4096 * 2, hstepA = (size_t)HALF * KD * 2, hstepB = HALF * 2;
  const unsigned ldsw = (unsigned)wid * 1024u;
  const int aoff = lds_byte(wr * 64 + fr, fq * 8);
  const int tq = (lane & 15) >> 2, tp = lane & 3;
  int boff[2];
#pragma unroll
  for (int n = 0; n < 2; ++n) boff[n] = (8 * fq + tq) * 256 + (((4 * wc + 2 * n + (tp >> 1)) ^ (2 * tq + 8 * (fq & 1))) * 16) + (tp & 1) * 8;
#define SA(b, h) (((b) * 2 + (h)) * HTB)
#define SB(b, h) ((4 + (b) * 2 + (h)) * HTB)
#define STAGE(bufoff, gbase, voff) do { _Pragma("unroll") for (int _i = 0; _i < 2; ++_i) \
    attb::dma16((gbase).srd, (voff)[_i], (gbase).off, lds0 + (unsigned)(bufoff) + ldsw + _i * 8192u); } while (0)
#define LDA(dst, b, h) do { _Pragma("unroll") for (int m = 0; m < 4; ++m) _Pragma("unroll") for (int k = 0; k < 2; ++k) dst[m][k] = *(const LAS bf16x8*)(lds + SA(b, h) + aoff + m * 2048 + k * 1024); } while (0)
#define LDB(dst, b, h) do { _Pragma("unroll") for (int n = 0; n < 2; ++n) _Pragma("unroll") for (int k = 0; k < 2; ++k) \
    dst[n][k] = cat8(tr_read(lds + SB(b, h) + boff[n] + k * 8192), tr_read(lds + SB(b, h) + boff[n] + k * 8192 + 1024)); } while (0)
#define MMA(ai, bj, At, Bt) do { __builtin_amdgcn_s_setprio(1); _Pragma("unroll") for (int m = 0; m < 4; ++m) _Pragma("unroll") for (int n = 0; n < 2; ++n) _Pragma("unroll") for (int k = 0; k < 2; ++k) \
    acc[ai][bj][m][n] = ISK ? __builtin_amdgcn_mfma_f32_16x16x32_bf16(Bt[n][k], At[m][k], acc[ai][bj][m][n], 0, 0, 0) : __builtin_amdgcn_mfma_f32_16x16x32_bf16(At[m][k], Bt[n][k], acc[ai][bj][m][n], 0, 0, 0); \
    __builtin_amdgcn_s_setprio(0); } while (0)
#define WAIT_V(n) asm volatile("s_waitcnt vmcnt(" #n ")" ::: "memory")
#define WAIT_L(n) asm volatile("s_waitcnt lgkmcnt(" #n ")" ::: "memory")
#define BAR __builtin_amdgcn_s_barrier()
  f32x4 acc[2][2][4][2];
#pragma unroll
  for (int a = 0; a < 2; ++a)
#pragma unroll
    for (int b = 0; b < 2; ++b)
#pragma unroll
      for (int m = 0; m < 4; ++m)
#pragma unroll
        for (int n = 0; n < 2; ++n) acc[a][b][m][n] = (f32x4){0.f, 0.f, 0.f, 0.f};
  bf16x8 At[4][2], B0[2][2], B1[2][2];
  struct GP { __amdgpu_buffer_rsrc_t srd; unsigned off; DI GP operator+(size_t d) const { return GP{srd, off + (unsigned)d}; } };
  const unsigned lds0 = (unsigned)(uintptr_t)lds;
  const GP cA{mk_srd(Ckv, 2048 * KD * 2), (unsigned)(pm * 256 * KD * 2)};
  const GP cB{mk_srd(W, KD * 4096 * 2), (unsigned)(pnl * 512)};
  LGKM0();
  if (wr == 1) BAR;
  WAIT_V(2); BAR;
  STAGE(SB(1, 0), cB + kstepB, voffB); STAGE(SA(1, 0), cA + kstepA, voffA); STAGE(SB(1, 1), cB + hstepB + kstepB, voffB);
  WAIT_V(6); BAR;
  flag();
  for (int t = 0; t < NT - 2; t += 2) {
    const GP a1 = cA + (size_t)(t + 1) * kstepA;
    const GP a2 = cA + (size_t)(t + 2) * kstepA; const GP b2 = cB + (size_t)(t + 2) * kstepB;
    const GP a3 = a2 + kstepA; const GP b3 = b2 + kstepB;
    LDB(B0, 0, 0); LDB(B1, 0, 1); SBAR(); LDA(At, 0, 0); STAGE(SA(1, 1), a1 + hstepA, voffA);
    WAIT_V(8); WAIT_L(0); BAR; MMA(0, 0, At, B0); MMA(0, 1, At, B1); BAR; SBAR();
    LDA(At, 0, 1); STAGE(SB(0, 0), b2, voffB); STAGE(SB(0, 1), b2 + hstepB, voffB); STAGE(SA(0, 0), a2, voffA);
    WAIT_V(8); WAIT_L(0); BAR; MMA(1, 0, At, B0); MMA(1, 1, At, B1); BAR; SBAR();
    LDB(B0, 1, 0); LDB(B1, 1, 1); SBAR(); LDA(At, 1, 0); STAGE(SA(0, 1), a2 + hstepA, voffA);
    WAIT_V(8); WAIT_L(0); BAR; MMA(0, 0, At, B0); MMA(0, 1, At, B1); BAR; SBAR();
    LDA(At, 1, 1); STAGE(SB(1, 0), b3, voffB); STAGE(SB(1, 1), b3 + hstepB, voffB); STAGE(SA(1, 0), a3, voffA);
    WAIT_V(8); WAIT_L(0); BAR; MMA(1, 0, At, B0); MMA(1, 1, At, B1); BAR; SBAR();
  }
  {
    const GP a1 = cA + (size_t)(NT - 1) * kstepA;
    LDB(B0, 0, 0); LDB(B1, 0, 1); SBAR(); LDA(At, 0, 0); STAGE(SA(1, 1), a1 + hstepA, voffA);
    WAIT_V(8); WAIT_L(0); BAR; MMA(0, 0, At, B0); MMA(0, 1, At, B1); BAR; SBAR();
    LDA(At, 0, 1);
    WAIT_V(2); WAIT_L(0); BAR; MMA(1, 0, At, B0); MMA(1, 1, At, B1); BAR; SBAR();
    LDB(B0, 1, 0); LDB(B1, 1, 1); SBAR(); LDA(At, 1, 0);
    WAIT_V(0); WAIT_L(0); BAR; MMA(0, 0, At, B0); MMA(0, 1, At, B1); BAR; SBAR();
    LDA(At, 1, 1);
    WAIT_L(0); BAR; MMA(1, 0, At, B0); MMA(1, 1, At, B1); BAR; SBAR();
  }
  if (wr == 0) BAR;
  asm volatile("s_waitcnt lgkmcnt(0)\n\ts_barrier" ::: "memory");
  after_loop();
  if (ISK) {
    lanebase = (unsigned)(((2 * pnl) * 32 + pm * 4 + wr) * 32768 + (4 * wc + fq) * 1024 + fr * 16);
#pragma unroll
    for (int ai = 0; ai < 2; ++ai)
#pragma unroll
      for (int bj = 0; bj < 2; ++bj)
#pragma unroll
        for (int m = 0; m < 4; ++m) {
          const f32x4 a0 = acc[ai][bj][m][0], a1 = acc[ai][bj][m][1];
          auto r0 = __builtin_amdgcn_permlane32_swap(cvtpk(a0[0], a0[1]), cvtpk(a1[0], a1[1]), false, false);
          auto r1 = __builtin_amdgcn_permlane32_swap(cvtpk(a0[2], a0[3]), cvtpk(a1[2], a1[3]), false, false);
          pk[(ai * 2 + bj) * 4 + m] = (u32x4){r0[0], r1[0], r0[1], r1[1]};
        }
  } else {
    lanebase = (unsigned)(((2 * pnl) * 32 + pm * 4 + wr) * 32768 + 16384 + fq * 2048 + (wc * 32 + fr) * 16);
#pragma unroll
    for (int ai = 0; ai < 2; ++ai)
#pragma unroll
      for (int bj = 0; bj < 2; ++bj)
#pragma unroll
        for (int u = 0; u < 2; ++u)
#pragma unroll
          for (int n = 0; n < 2; ++n) {
            const f32x4 a0 = acc[ai][bj][2 * u][n], a1 = acc[ai][bj][2 * u + 1][n];
            auto r0 = __builtin_amdgcn_permlane32_swap(cvtpk(a0[0], a0[1]), cvtpk(a1[0], a1[1]), false, false);
            auto r1 = __builtin_amdgcn_permlane32_swap(cvtpk(a0[2], a0[3]), cvtpk(a1[2], a1[3]), false, false);
            pk[(ai * 2 + bj) * 4 + u * 2 + n] = (u32x4){r0[0], r1[0], r0[1], r1[1]};
          }
  }
#pragma unroll
  for (int i = 0; i < 8; ++i)
#pragma unroll
    for (int e = 0; e < 4; ++e) { pkA[4 * i + e] = pk[i][e]; pkB[4 * i + e] = pk[8 + i][e]; }
#undef SA
#undef SB
#undef STAGE
#undef LDA
#undef LDB
#undef MMA
}
DI void kv_store(__amdgpu_buffer_rsrc_t srdO, unsigned lanebase, bool isk, int i, const u32x4& v) {
  const int ai = i >> 3, bj = (i >> 2) & 1, z = i & 3;
  const unsigned so = (unsigned)(bj * (32 * 32768) + ai * (2 * 32768)) + (isk ? (unsigned)(z * 256) : (unsigned)((z >> 1) * 8192 + (z & 1) * 256));
  __builtin_amdgcn_raw_buffer_store_b128(v, srdO, lanebase + so, 0, 16);
  asm volatile("s_nop 1" ::: "memory");
}
}

namespace qa {
constexpr int STG = 24576;
constexpr int QT = 147456;
template <bool FIRST>
DI void proj(LAS char* lds, const u16* __restrict__ X, const u16* __restrict__ wq, int rb, int hg) {
  const int tid = threadIdx.x, wid = __builtin_amdgcn_readfirstlane(tid >> 6), lane = tid & 63, fr = lane & 15, g = lane >> 4, q = (lane & 15) >> 2, p = lane & 3;
  const int mh = wid & 1, kk = (wid >> 1) & 1, nh = wid >> 2;
  const unsigned l0 = (unsigned)(uintptr_t)lds;
  const __amdgpu_buffer_rsrc_t srdX = mk_srd(X, S * 1152), srdW = mk_srd(wq, 576 * 1152);
  const int csh = (hg & 1) * 2;
  unsigned off[3]; bool isx[3];
#pragma unroll
  for (int i = 0; i < 3; ++i) { const int pc = wid + 8 * i;
    if (pc < 4) { const int row = 8 * pc + (lane >> 3); off[i] = (unsigned)((rb * 32 + row) * 1152 + (((lane & 7) ^ ((row >> 1) & 7)) * 16)); isx[i] = true; }
    else { const int L = (pc - 4) * 64 + lane, k = L / 20, cp = L - 20 * k, ch = cp ^ (2 * ((k >> 2) & 1));
      int colb = hg * 288 - csh * 16 + ch * 16; if (colb > 1152 - 16) colb = 1152 - 16;
      off[i] = (unsigned)(k * 1152 + colb); isx[i] = false; } }
  auto issue = [&](int kt) {
    const unsigned sb = l0 + (unsigned)(kt % 6) * STG;
#pragma unroll
    for (int i = 0; i < 3; ++i) { const int pc = wid + 8 * i;
      if (i == 0 && wid < 4) attb::dma16(srdX, off[i], (unsigned)kt * 128u, sb + (unsigned)pc * 1024u);
      else attb::dma16(srdW, off[i], (unsigned)kt * (64u * 1152u), sb + (unsigned)pc * 1024u); } };
  (void)isx;
  const int row = 16 * mh + fr;
  const int xrd = row * 128 + (((4 * kk + g) ^ ((row >> 1) & 7)) * 16);
  int wrd[2][2];
#pragma unroll
  for (int t = 0; t < 2; ++t) { const int k = 32 * kk + 8 * g + 4 * t + q;
#pragma unroll
    for (int par = 0; par < 2; ++par) { const int ch = csh + 2 * par + (p >> 1), cp = (ch & ~3) + ((ch & 3) ^ (2 * ((k >> 2) & 1))); wrd[t][par] = 4096 + k * 320 + cp * 16 + (p & 1) * 8; } }
  f32x4 acc[5];
#pragma unroll
  for (int n = 0; n < 5; ++n) acc[n] = (f32x4){0.f, 0.f, 0.f, 0.f};
  auto comp = [&](int kt) {
    LAS const char* sb = lds + (kt % 6) * STG;
    const bf16x8 xf = *(const LAS bf16x8*)(sb + xrd);
    bf16x8 wf[5];
#pragma unroll
    for (int n = 0; n < 5; ++n) { const int nb = nh * 5 + (n < 4 || nh == 0 ? n : 3);
      wf[n] = cat8(tr_read(sb + wrd[0][nb & 1] + (nb >> 1) * 64), tr_read(sb + wrd[1][nb & 1] + (nb >> 1) * 64)); }
#pragma unroll
    for (int n = 0; n < 5; ++n) acc[n] = MFMA16(wf[n], xf, acc[n]); };
  if constexpr (FIRST) {
#pragma unroll
    for (int kt = 0; kt < 6; ++kt) issue(kt);
    return; }
  LGKM0();
  WAITV_BAR(9); comp(0); comp(1); comp(2);
  asm volatile("s_waitcnt lgkmcnt(0)\n\ts_barrier" ::: "memory");
  issue(6); issue(7); issue(8);
  WAITV_BAR(9); comp(3); comp(4); comp(5);
  WAITV_BAR(0); comp(6); comp(7); comp(8);
  asm volatile("s_waitcnt lgkmcnt(0)\n\ts_barrier" ::: "memory");
  LAS char* rb_ = lds + (mh * 2 + nh) * 5120 + lane * 16;
  if (kk == 1) {
#pragma unroll
    for (int n = 0; n < 5; ++n) *(LAS f32x4*)(rb_ + n * 1024) = acc[n]; }
  asm volatile("s_waitcnt lgkmcnt(0)\n\ts_barrier" ::: "memory");
  if (kk == 0) {
#pragma unroll
    for (int n = 0; n < 5; ++n) if (n < 4 || nh == 0) {
      const f32x4 v = (acc[n] + *(const LAS f32x4*)(rb_ + n * 1024)) * QSCALE_A;
      *(LAS u32x2*)(lds + QT + row * 304 + (16 * (nh * 5 + n) + 4 * g) * 2) = (u32x2){cvtpk(v[0], v[1]), cvtpk(v[2], v[3])}; } }
  asm volatile("s_waitcnt lgkmcnt(0)\n\ts_barrier" ::: "memory");
}
}
namespace atta {
constexpr float MARGIN = 48.f;
constexpr int OA_RB = 72 * 512;
#define STA(i) do {} while (0)
template <class HOOK>
DI void run(LAS char* lds, const u16* __restrict__ QA, const char* __restrict__ KVA  , char* __restrict__ OA, int rb, int hg, HOOK&& hook, u32x4& res0, unsigned& res1) {
  const int tid = threadIdx.x, wid = __builtin_amdgcn_readfirstlane(tid >> 6), lane = tid & 63, r32 = lane & 31, hi = lane >> 5;
  const unsigned lds0 = (unsigned)(uintptr_t)lds;
  const int head = hg * 8 + wid;
  STA(0);
  const __amdgpu_buffer_rsrc_t srd = mk_srd(KVA, 151552);
  auto piece = [&](int gq, int i) {
    int j = wid + 8 * i; if (j > 36) j = 36;
    const unsigned off = j < 8 ? (unsigned)(gq * 8 + j) * 1024u : (j < 16 ? 32768u + (unsigned)(gq * 8 + j - 8) * 1024u : (j < 18 ? 65536u + (unsigned)(gq * 2 + j - 16) * 1024u : 73728u + (unsigned)(gq * 19 + j - 18) * 1024u));
    attb::dma16(srd, (unsigned)lane * 16u, (unsigned)__builtin_amdgcn_readfirstlane(off), (unsigned)__builtin_amdgcn_readfirstlane(lds0 + off));
  };
#pragma unroll
  for (int i = 0; i < 5; ++i) piece(0, i);
  u32x4 q0, q1 = {0u, 0u, 0u, 0u};
  { LAS const unsigned* p = (LAS const unsigned*)(lds + qa::QT + r32 * 304 + wid * 36 + hi * 16); q0 = (u32x4){p[0], p[1], p[2], p[3]};
    const unsigned v = *(LAS const unsigned*)(lds + qa::QT + r32 * 304 + wid * 36 + 32); q1[0] = hi == 0 ? v : 0u; }
  const bf16x8 Q0 = __builtin_bit_cast(bf16x8, q0), Q1 = __builtin_bit_cast(bf16x8, q1);
  LAS const char* k0_l = lds + hi * 32768 + r32 * 16;
  LAS const char* k1_l = lds + 65536 + r32 * 4;
  LAS const char* v_l = lds + 73728 + hi * 304 + (r32 < 18 ? r32 : 18) * 16;
  asm volatile("" : "+v"(k0_l), "+v"(k1_l), "+v"(v_l));
  unsigned zr1 = 0u, zr2 = 0u, zr3 = 0u; asm volatile("" : "+v"(zr1), "+v"(zr2), "+v"(zr3));
#define KA0(t) (*(const LAS bf16x8*)(k0_l + (t) * 512))
#define KA1(t) __builtin_bit_cast(bf16x8, (u32x4){*(const LAS unsigned*)(k1_l + (t) * 128), 0u, 0u, 0u})
#define VA(t, ks) (*(const LAS bf16x8*)(v_l + ((t) * 4 + (ks) * 2) * 304))
  f32x16 O, NEGM, SA, SB;
#pragma unroll
  for (int r = 0; r < 16; ++r) O[r] = 0.f;
  u32x4 PA0, PA1, PB0, PB1;
  { bf16x8 t0_ = Q0, t1_ = Q1; PIN(t0_); PIN(t1_); }
  asm volatile("s_waitcnt vmcnt(0) lgkmcnt(0)\n\ts_barrier" ::: "memory");
  STA(1);
  {
    f32x16 z;
#pragma unroll
    for (int r = 0; r < 16; ++r) z[r] = 0.f;
    SA = MFMA32(KA0(0), Q0, z); SA = MFMA32(KA1(0), Q1, SA);
    float m = SA[0];
#pragma unroll
    for (int r = 1; r < 16; ++r) m = __builtin_fmaxf(m, SA[r]);
    { auto rr = __builtin_amdgcn_permlane32_swap(__float_as_uint(m), __float_as_uint(m), false, false); m = __builtin_fmaxf(__uint_as_float(rr[0]), __uint_as_float(rr[1])); }
    const float nm = -(m + MARGIN);
#pragma unroll
    for (int r = 0; r < 16; ++r) { NEGM[r] = nm; SA[r] += nm; }
#pragma unroll
    for (int e = 0; e < 4; ++e) { PB0[e] = 0u; PB1[e] = 0u; }
  }
  bf16x8 kf0 = KA0(1), kf1 = KA1(1), vf0 = VA(0, 0), vf1 = VA(0, 1);
  auto step = [&](f32x16& X, f32x16& Y, u32x4& Pc0, u32x4& Pc1, const u32x4& Pp0, const u32x4& Pp1, int t) {
    const int tn = t + 2;
    auto ex4 = [&](int b) { X[b] = EXP2(X[b]); X[b + 1] = EXP2(X[b + 1]); X[b + 2] = EXP2(X[b + 2]); X[b + 3] = EXP2(X[b + 3]); PIN(X); };
    SBAR();
    Y = MFMA32(kf0, Q0, NEGM); PIN(Y); kf0 = KA0(tn); ex4(0); SBAR();
    Y = MFMA32(kf1, Q1, Y); PIN(Y); kf1 = KA1(tn); Pc0[0] = attb::ppk(X[0], X[1]); Pc0[1] = attb::ppk(X[2], X[3]); PIN(Pc0); ex4(4); SBAR();
    O = MFMA32(vf0, PF(Pp0), O); PIN(O); vf0 = VA(t, 0); Pc0[2] = attb::ppk(X[4], X[5]); Pc0[3] = attb::ppk(X[6], X[7]); PIN(Pc0); ex4(8); SBAR();
    O = MFMA32(vf1, PF(Pp1), O); PIN(O); vf1 = VA(t, 1); Pc1[0] = attb::ppk(X[8], X[9]); Pc1[1] = attb::ppk(X[10], X[11]); PIN(Pc1); ex4(12); SBAR();
    Pc1[2] = attb::ppk(X[12], X[13]); Pc1[3] = attb::ppk(X[14], X[15]); PIN(Pc1); SBAR();
  };
  STA(2);
  LGKM0();
  attb::sfor<16>([&](auto I) { constexpr int t = 2 * I;
    if constexpr (t == 14) { WAITV_BAR_NL(13); }
    if constexpr (t == 30) { WAITV_BAR_NL(15); }
    hook(I);
    if constexpr (t < 15) piece(1 + t / 5, t % 5);
    step(SA, SB, PA0, PA1, PB0, PB1, t);
    if constexpr (t + 1 < 15) piece(1 + (t + 1) / 5, (t + 1) % 5);
    step(SB, SA, PB0, PB1, PA0, PA1, t + 1); });
  attb::sfor<6>([&](auto I) { constexpr int t0 = 32 + 4 * I;
    if constexpr (t0 == 44) {}
    hook(std::integral_constant<int, 16 + I>{});
#pragma unroll 1
    for (int t = t0; t < t0 + 2; t += 2) { step(SA, SB, PA0, PA1, PB0, PB1, t); step(SB, SA, PB0, PB1, PA0, PA1, t + 1); }
    if constexpr (t0 == 44) { WAITV_BAR_NL(12); }
    step(SA, SB, PA0, PA1, PB0, PB1, t0 + 2); step(SB, SA, PB0, PB1, PA0, PA1, t0 + 3); });
#pragma unroll 1
  for (int t = 56; t < 64; t += 2) { step(SA, SB, PA0, PA1, PB0, PB1, t); step(SB, SA, PB0, PB1, PA0, PA1, t + 1); }
  STA(3);
  asm volatile("s_waitcnt vmcnt(0)" ::: "memory");
  O = MFMA32(vf0, PF(PB0), O); O = MFMA32(vf1, PF(PB1), O);
  {
    float lq = O[10];
    { auto rr = __builtin_amdgcn_permlane32_swap(__float_as_uint(lq), __float_as_uint(lq), false, false); lq = __uint_as_float(rr[0]); }
    float chk = lq;
#pragma unroll
    for (int r = 0; r < 10; ++r) chk += O[r] * 0.f;
    const bool bad = !(chk < 1.0e37f) || !(lq > 0.f);
    if (__builtin_expect(__any(bad), 0)) {
      float m = -1.0e30f;
#pragma unroll
      for (int r = 0; r < 16; ++r) O[r] = 0.f;
#pragma unroll 1
      for (int t = 0; t < 64; ++t) {
        LAS const char* k0p = lds + hi * 32768 + r32 * 16 + t * 512; LAS const char* k1p = lds + 65536 + r32 * 4 + t * 128; LAS const char* vp = lds + 73728 + hi * 304 + (r32 < 18 ? r32 : 18) * 16 + t * 4 * 304;
#pragma unroll
        for (int r = 0; r < 16; ++r) SA[r] = 0.f;
        SA = MFMA32(*(const LAS bf16x8*)k0p, Q0, SA);
        SA = MFMA32(__builtin_bit_cast(bf16x8, (u32x4){*(const LAS unsigned*)k1p, 0u, 0u, 0u}), Q1, SA);
        float mx = SA[0];
#pragma unroll
        for (int r = 1; r < 16; ++r) mx = __builtin_fmaxf(mx, SA[r]);
        { auto rr = __builtin_amdgcn_permlane32_swap(__float_as_uint(mx), __float_as_uint(mx), false, false); mx = __builtin_fmaxf(__uint_as_float(rr[0]), __uint_as_float(rr[1])); }
        const float mn = __builtin_fmaxf(m, mx), alpha = EXP2(m - mn);
        m = mn;
#pragma unroll
        for (int r = 0; r < 16; ++r) { SA[r] = EXP2(SA[r] - mn); O[r] *= alpha; }
        PA0 = (u32x4){cvtpk(SA[0], SA[1]), cvtpk(SA[2], SA[3]), cvtpk(SA[4], SA[5]), cvtpk(SA[6], SA[7])};
        PA1 = (u32x4){cvtpk(SA[8], SA[9]), cvtpk(SA[10], SA[11]), cvtpk(SA[12], SA[13]), cvtpk(SA[14], SA[15])};
        O = MFMA32(*(const LAS bf16x8*)vp, PF(PA0), O); O = MFMA32(*(const LAS bf16x8*)(vp + 2 * 304), PF(PA1), O);
      }
    }
  }
  float l = O[10];
  { auto rr = __builtin_amdgcn_permlane32_swap(__float_as_uint(l), __float_as_uint(l), false, false); l = __uint_as_float(rr[0]); }
  const float linv = 1.0f / l;
#pragma unroll
  for (int e = 0; e < 4; ++e) res0[e] = cvtpk(O[2 * e] * linv, O[2 * e + 1] * linv);
  res1 = cvtpk(O[8] * linv, O[9] * linv);
  (void)OA;
}
DI void store(char* __restrict__ OA, int rb, int hg, const u32x4& res0, unsigned res1) {
  const int tid = threadIdx.x, wid = __builtin_amdgcn_readfirstlane(tid >> 6), lane = tid & 63, r32 = lane & 31, hi = lane >> 5, head = hg * 8 + wid;
  const __amdgpu_buffer_rsrc_t srdO = mk_srd((OA + (size_t)rb * OA_RB), OA_RB);
  st16(srdO, (unsigned)(r32 * 16 + (2 * head + hi) * 512), res0);
  __builtin_amdgcn_raw_buffer_store_b32(res1, srdO, hi == 0 ? (unsigned)((64 + (head >> 2)) * 512 + r32 * 16 + (head & 3) * 4) : 0x7fffffffu, 0, 16);
}
}

namespace outp {
constexpr int STG = 20480, NSTG = 4, LDS_BYTES = 8 * 9216 + 4096;
constexpr int KT_A5 = 9;
#define OUTP_SPLIT 20
constexpr int KSPL = OUTP_SPLIT;
DI void ldA(bf16x8& r, __amdgpu_buffer_rsrc_t srd, unsigned voff, unsigned soff) { asm volatile("buffer_load_dwordx4 %0, %1, %2, %3 offen" : "=&v"(r) : "v"(voff), "s"(srd), "s"(soff) : "memory"); }

template <bool ISB, bool HI, int REM, class DEP>
DI void kloop(f32x4 (&acc)[2][9], LAS char* lds, const char* __restrict__ Asrc, const u16* __restrict__ W, int ti, int tj, int kt0, int nkt, DEP&& dep) {
  const int tid = threadIdx.x, wid = __builtin_amdgcn_readfirstlane(tid >> 6), lane = tid & 63, wm = wid & 3, wk = HI ? 1 : 0, fr = lane & 15, g = lane >> 4, q = (lane & 15) >> 2, p = lane & 3;
  constexpr int ARB = ISB ? CTX_RB : 72 * 512;
  const unsigned lds0 = (unsigned)(uintptr_t)lds;
  const __amdgpu_buffer_rsrc_t srdA = mk_srd((Asrc + (size_t)(ti * 4 + wm) * ARB), ARB);
  const __amdgpu_buffer_rsrc_t srdW = mk_srd(W, (ISB ? 4096 : 576) * 1152);
  const int csh = (tj & 1) * 2;
  const unsigned aoff = (unsigned)((4 * wk + g) * 512 + fr * 16);
  unsigned woff[3], woff8[3];
#pragma unroll
  for (int i = 0; i < 3; ++i) { const int pc = i < 2 ? 2 * wid + i : 16 + (wid & 3);
    const int L = pc * 64 + lane, k = L / 20, cp = L - 20 * k, ch = cp ^ (2 * ((k >> 2) & 1));
    int colb = tj * 288 - csh * 16 + ch * 16; if (colb > 1152 - 16) colb = 1152 - 16;
    const int krow = ISB ? k : 18 * (k >> 4) + (k & 15);
    woff[i] = (unsigned)(krow * 1152 + colb);
    const int u = 4 * (k >> 5) + 2 * ((k >> 4) & 1) + ((k >> 2) & 1), j = 4 * ((k >> 3) & 1) + (k & 3);
    woff8[i] = (unsigned)((18 * (4 * u + (j >> 1)) + 16 + (j & 1)) * 1152 + colb); }
  constexpr unsigned wstep = (ISB ? 64u : 72u) * 1152u;
  auto wpiece = [&](int n, int i) {
    const unsigned sb = lds0 + (unsigned)(n & (NSTG - 1)) * STG; const unsigned kt = (unsigned)(kt0 + n);
    const bool sp = !ISB && kt == 8;
    if (i < 2) attb::dma16(srdW, sp ? woff8[i] : woff[i], sp ? 0u : kt * wstep, sb + (unsigned)wid * 2048u + i * 1024u);
    else if (!HI) attb::dma16(srdW, sp ? woff8[2] : woff[2], sp ? 0u : kt * wstep, sb + 16384u + (unsigned)wid * 1024u);
  };
  auto wpiece2 = [&](int slot_n, int n, int i) {
    const unsigned sb = lds0 + (unsigned)(slot_n & (NSTG - 1)) * STG; const unsigned kt = (unsigned)(kt0 + n);
    const bool sp = !ISB && kt == 8;
    if (i < 2) attb::dma16(srdW, sp ? woff8[i] : woff[i], sp ? 0u : kt * wstep, sb + (unsigned)wid * 2048u + i * 1024u);
    else if (!HI) attb::dma16(srdW, sp ? woff8[2] : woff[2], sp ? 0u : kt * wstep, sb + 16384u + (unsigned)wid * 1024u);
  };
  auto aload = [&](bf16x8 (&a)[2], int n) { const unsigned so = (unsigned)(kt0 + n) * 4096u; ldA(a[0], srdA, aoff, so); ldA(a[1], srdA, aoff + 256u, so); };
  int wrd[2][2];
#pragma unroll
  for (int t = 0; t < 2; ++t) {
    const int k = 32 * wk + 16 * (g >> 1) + 8 * t + 4 * (g & 1) + q;
#pragma unroll
    for (int par = 0; par < 2; ++par) {
      const int ch = csh + 2 * par + (p >> 1);
      const int cp = (ch & ~3) + ((ch & 3) ^ (2 * ((k >> 2) & 1)));
      wrd[t][par] = k * 320 + cp * 16 + (p & 1) * 8; } }
  auto rdw = [&](LAS const char* sb, int nb) -> bf16x8 { return cat8(tr_read(sb + wrd[0][nb & 1] + (nb >> 1) * 64), tr_read(sb + wrd[1][nb & 1] + (nb >> 1) * 64)); };
  constexpr int RQ = HI ? 4 : 5;
#define K3W(N) do { if constexpr ((N) == 0) { WAITV_BAR(0); } else if constexpr ((N) == 1) { if constexpr (HI) { WAITV_BAR(4); } else { WAITV_BAR(5); } } else if constexpr ((N) == 2) { if constexpr (HI) { WAITV_BAR(8); } else { WAITV_BAR(10); } } \
                    else { if constexpr (HI) { WAITV_BAR(12); } else { WAITV_BAR(15); } } } while (0)
  bf16x8 A0[2], A1[2], A2[2], A3[2], F[9];
#pragma unroll
  for (int u = 0; u < 4; ++u)
#pragma unroll
    for (int i = 0; i < 3; ++i) wpiece(u, i);
  dep();
  aload(A0, 0); aload(A1, 1); aload(A2, 2);
  LGKM0();
  WAITV_BAR(0);
#pragma unroll
  for (int i = 0; i < 9; ++i) F[i] = rdw(lds, i);
  auto iter = [&](auto MODEC, bf16x8 (&Ac)[2], bf16x8 (&An)[2], int n) { constexpr int MODE = MODEC;
    if constexpr (MODE <= 1) K3W(2);
    else if constexpr (MODE == 2) { if constexpr (HI) { WAITV_BAR(6); } else { WAITV_BAR(7); } }
    else if constexpr (MODE == 3) { WAITV_BAR(2); }
    else { WAITV_BAR(0); }
    asm volatile("" : "+v"(Ac[0]), "+v"(Ac[1]));
    LAS const char* sb = lds + ((n + 1) & (NSTG - 1)) * STG;
    SBAR();
    attb::sfor<9>([&](auto I) { constexpr int j = I;
      acc[0][j] = MFMA16(F[j], Ac[0], acc[0][j]); PIN(acc[0][j]);
      acc[1][j] = MFMA16(F[j], Ac[1], acc[1][j]); PIN(acc[1][j]);
      if constexpr (MODE < 4) F[j] = rdw(sb, j);
      if constexpr (MODE <= 1 && j == (HI ? 5 : 0)) aload(An, n + 3);
      if constexpr (MODE == 0 && j >= (HI ? 6 : 1) && j < (HI ? 9 : 4)) wpiece(n + 4, j - (HI ? 6 : 1));
      SBAR(); });
  };
  using M0 = std::integral_constant<int, 0>; using M1 = std::integral_constant<int, 1>; using M2 = std::integral_constant<int, 2>; using M3 = std::integral_constant<int, 3>; using M4 = std::integral_constant<int, 4>;
  int n = 0;
  if constexpr (REM == 1) { iter(M0{}, A0, A3, 0); n = 1;
#pragma unroll 1
    for (; n + 4 < nkt; n += 4) { iter(M0{}, A1, A0, n); iter(M0{}, A2, A1, n + 1); iter(M0{}, A3, A2, n + 2); iter(M0{}, A0, A3, n + 3); }
    iter(M1{}, A1, A0, n); iter(M2{}, A2, A1, n + 1); iter(M3{}, A3, A2, n + 2); iter(M4{}, A0, A3, n + 3);
  } else if constexpr (REM == 2) { iter(M0{}, A0, A3, 0); iter(M0{}, A1, A0, 1); n = 2;
#pragma unroll 1
    for (; n + 4 < nkt; n += 4) { iter(M0{}, A2, A1, n); iter(M0{}, A3, A2, n + 1); iter(M0{}, A0, A3, n + 2); iter(M0{}, A1, A0, n + 3); }
    iter(M1{}, A2, A1, n); iter(M2{}, A3, A2, n + 1); iter(M3{}, A0, A3, n + 2); iter(M4{}, A1, A0, n + 3);
  } else {
#pragma unroll 1
    for (; n + 4 < nkt; n += 4) { iter(M0{}, A0, A3, n); iter(M0{}, A1, A0, n + 1); iter(M0{}, A2, A1, n + 2); iter(M0{}, A3, A2, n + 3); }
    iter(M1{}, A0, A3, n); iter(M2{}, A1, A0, n + 1); iter(M3{}, A2, A1, n + 2); iter(M4{}, A3, A2, n + 3);
  }
  asm volatile("s_waitcnt lgkmcnt(0)\n\ts_barrier" ::: "memory");
#undef K3W
}
DI void pair_reduce(f32x4 (&acc)[2][9], f32x4 (&r)[9], LAS char* lds) {
  const int tid = threadIdx.x, wid = __builtin_amdgcn_readfirstlane(tid >> 6), lane = tid & 63, wk = wid >> 2;
  LAS char* mine = lds + wid * 9216 + lane * 16; LAS char* other = lds + (wid ^ 4) * 9216 + lane * 16;
#pragma unroll
  for (int nb = 0; nb < 9; ++nb) *(LAS f32x4*)(mine + nb * 1024) = wk ? acc[0][nb] : acc[1][nb];
  asm volatile("s_waitcnt lgkmcnt(0)\n\ts_barrier" ::: "memory");
#pragma unroll
  for (int nb = 0; nb < 9; ++nb) r[nb] = (wk ? acc[1][nb] : acc[0][nb]) + *(const LAS f32x4*)(other + nb * 1024);
  asm volatile("s_waitcnt lgkmcnt(0)\n\ts_barrier" ::: "memory");
}
template <bool HI>
DI void body(LAS char* lds, char* __restrict__ ws, const u16* __restrict__ WoB, const u16* __restrict__ WoA, u16* __restrict__ out, int ti, int tj, int s, gu32* sy, unsigned tag) {
  const int tid = threadIdx.x, wid = __builtin_amdgcn_readfirstlane(tid >> 6), lane = tid & 63, wm = wid & 3, wk = HI ? 1 : 0, fr = lane & 15, g = lane >> 4;
  f32x4 acc[2][9], r[9];
  const int myrow = ti * 128 + 32 * wm + 16 * wk + fr;
  auto zero = [&]() {
#pragma unroll
    for (int m = 0; m < 2; ++m)
#pragma unroll
      for (int n = 0; n < 9; ++n) acc[m][n] = (f32x4){0.f, 0.f, 0.f, 0.f}; };
  auto st = [&](const f32x4& v, int nb, int cbase) { *(u32x2*)(out + (size_t)myrow * 1152 + cbase + tj * 144 + 16 * nb + 4 * g) = (u32x2){cvtpk(v[0], v[1]), cvtpk(v[2], v[3])}; };
  ST(ws, 3, 0);
  if (s == 3) {
    f32x4 r5[9];
    zero();
    kloop<false, HI, 1>(acc, lds, ws + WS_OA, WoA, ti, tj, 0, KT_A5, [&]() { if (wid == 0) { sy_wait<1>(sy + SY_OA + ti * 16, 16, tag, lane); sy_acquire(); } asm volatile("s_barrier" ::: "memory"); });
    pair_reduce(acc, r5, lds);
#pragma unroll
    for (int n = 0; n < 9; ++n) st(r5[n], n, 0);
    return;
  }
  ST(ws, 3, 1);
  zero();
  auto depc = [&]() { if (wid == 0) { sy_wait<1>(sy + SY_CTX + ti * 32, 32, tag, lane); sy_acquire(); } asm volatile("s_barrier" ::: "memory"); };
  if (s < 2) kloop<true, HI, 1>(acc, lds, ws + WS_CTX, WoB, ti, tj, s * 21, 21, depc);
  else kloop<true, HI, 2>(acc, lds, ws + WS_CTX, WoB, ti, tj, 42, 22, depc);
  ST(ws, 3, 2);
  pair_reduce(acc, r, lds);
  const int tile = ti * 4 + tj;
  gu32* fp = sy + SY_PART + tile * 4;
  const __amdgpu_buffer_rsrc_t srdP = mk_srd((ws + WS_PART + (size_t)tile * (4 * 8 * 9216)), 4 * 8 * 9216);
  const unsigned ob = (unsigned)(wid * 9216 + lane * 16);
  attb::sfor<9>([&](auto I) { constexpr int f = I;
    if ((f % 3) != s) __builtin_amdgcn_raw_buffer_store_b128(__builtin_bit_cast(u32x4, r[f]), srdP, ob + s * 73728 + f * 1024, 0, 16); });
  asm volatile("s_waitcnt vmcnt(0)\n\ts_barrier" ::: "memory");
  ST(ws, 3, 3);
  if (tid == 0) sy_set(fp + s, tag);
  if (wid == 0) sy_wait<1>(fp, 3, tag, lane);
  asm volatile("s_barrier" ::: "memory");
  ST(ws, 3, 4);
  f32x4 v[3][3];
#pragma unroll
  for (int u = 0; u < 3; ++u) { const int f = s + 3 * u;
#pragma unroll
    for (int src = 0; src < 3; ++src) if (src != s) v[u][src] = __builtin_bit_cast(f32x4, __builtin_amdgcn_raw_buffer_load_b128(srdP, ob + src * 73728 + f * 1024, 0, 16)); }
  attb::sfor<9>([&](auto I) { constexpr int f = I, u = f / 3;
    if ((f % 3) == s) {
      f32x4 sum = {0.f, 0.f, 0.f, 0.f};
#pragma unroll
      for (int src = 0; src < 3; ++src) sum += (src == s) ? r[f] : v[u][src];
      st(sum, f, 576);
    } });
  ST(ws, 3, 5);
}
}
namespace kva {
template <class NEXT>
DI void tile(LAS char* lds, int blk, const u16* __restrict__ X, const u16* __restrict__ wk, const u16* __restrict__ wv, char* __restrict__ KA, NEXT&& next) {
  const int tid = threadIdx.x, wid = __builtin_amdgcn_readfirstlane(tid >> 6), lane = tid & 63, wm = wid & 3, kh = wid >> 2, fr = lane & 15, g = lane >> 4, q = (lane & 15) >> 2, p = lane & 3;
  const unsigned l0 = (unsigned)(uintptr_t)lds;
  const int row0 = 64 * blk;
  u32x4 vk[3], vv[3];
#pragma unroll
  for (int i = 0; i < 3; ++i) { const int c = tid + 512 * i; const int cc = c < 1296 ? c : 0;
    vk[i] = *(const u32x4*)((const char*)wk + (size_t)cc * 16); vv[i] = *(const u32x4*)((const char*)wv + (size_t)cc * 16); }
  asm volatile("" ::: "memory");
  const __amdgpu_buffer_rsrc_t srdX = mk_srd(X, S * 1152);
  { const int row = 8 * wid + (lane >> 3);
    const unsigned xo = (unsigned)((row0 + row) * 1152 + (((lane & 7) ^ ((row >> 1) & 7)) * 16));
#pragma unroll
    for (int kt = 0; kt < 9; ++kt) attb::dma16(srdX, xo, (unsigned)kt * 128u, l0 + (unsigned)kt * 8192u + (unsigned)wid * 1024u); }
  LAS unsigned* W32 = (LAS unsigned*)(lds + 73728);
#pragma unroll
  for (int i = 0; i < 7; ++i) { const int z = tid + 512 * i; if (z < 576 * 6) { const int k = z / 6; W32[k * 24 + 18 + (z - 6 * k)] = 0u; } }
#pragma unroll
  for (int i = 0; i < 3; ++i) { const int c = tid + 512 * i;
    if (c < 1296) {
#pragma unroll
      for (int e = 0; e < 4; ++e) { const int d = 4 * c + e, k = d / 9, pi = d - 9 * k; W32[k * 24 + pi] = vk[i][e]; W32[k * 24 + 9 + pi] = vv[i][e]; } } }
  f32x4 acc[3];
#pragma unroll
  for (int n = 0; n < 3; ++n) acc[n] = (f32x4){0.f, 0.f, 0.f, 0.f};
  WAITV_BAR(0);
  const int xrow = 16 * wm + fr;
  LAS const char* xb = lds + xrow * 128; const int xsw = (xrow >> 1) & 7;
  LAS const char* wb = lds + 73728 + (8 * g + q) * 96 + p * 8;
  auto comp = [&](int s) {
    const bf16x8 xf = *(const LAS bf16x8*)(xb + (s >> 1) * 8192 + (((4 * (s & 1) + g) ^ xsw) * 16));
    bf16x8 wf[3];
#pragma unroll
    for (int n = 0; n < 3; ++n) { LAS const char* w_ = wb + s * (32 * 96) + n * 32; wf[n] = cat8(tr_read(w_), tr_read(w_ + 4 * 96)); }
#pragma unroll
    for (int n = 0; n < 3; ++n) acc[n] = MFMA16(wf[n], xf, acc[n]); };
#pragma unroll
  for (int s = 0; s < 9; ++s) comp(kh * 9 + s);
  asm volatile("s_waitcnt lgkmcnt(0)\n\ts_barrier" ::: "memory");
  next();
  LAS char* rb = lds + 131072 + wm * 3072 + lane * 16;
  if (kh == 1) {
#pragma unroll
    for (int n = 0; n < 3; ++n) *(LAS f32x4*)(rb + n * 1024) = acc[n]; }
  asm volatile("s_waitcnt lgkmcnt(0)\n\ts_barrier" ::: "memory");
  LAS char* tb = lds + 147456;
  if (kh == 0) {
#pragma unroll
    for (int n = 0; n < 3; ++n) { const f32x4 v = acc[n] + *(const LAS f32x4*)(rb + n * 1024);
      *(LAS u32x2*)(tb + xrow * 96 + (16 * n + 4 * g) * 2) = (u32x2){cvtpk(v[0], v[1]), cvtpk(v[2], v[3])}; } }
  asm volatile("s_waitcnt lgkmcnt(0)\n\ts_barrier" ::: "memory");
  const __amdgpu_buffer_rsrc_t srdK = mk_srd(KA, 73728 + 77824);
  if (tid < 64) { const int r = tid, key = row0 + r; LAS const unsigned* s_ = (LAS const unsigned*)(tb + r * 96);
    st16(srdK, (unsigned)(key * 16), (u32x4){s_[0], s_[1], s_[2], s_[3]}); st16(srdK, (unsigned)(32768 + key * 16), (u32x4){s_[4], s_[5], s_[6], s_[7]}); st4g(KA + 65536 + key * 4, s_[8]); }
  if (tid >= 64 && tid < 64 + 152) { const int u = tid - 64, cl = u / 19, d = u - 19 * cl;
    unsigned w[4];
#pragma unroll
    for (int e = 0; e < 4; ++e) { const int j0 = 2 * e, j1 = 2 * e + 1;
      const int r0 = 16 * (cl >> 1) + 8 * (j0 >> 2) + 4 * (cl & 1) + (j0 & 3), r1 = 16 * (cl >> 1) + 8 * (j1 >> 2) + 4 * (cl & 1) + (j1 & 3);
      const unsigned lo = d < 18 ? *(LAS const u16*)(tb + r0 * 96 + (18 + d) * 2) : 0x3F80u, hi_ = d < 18 ? *(LAS const u16*)(tb + r1 * 96 + (18 + d) * 2) : 0x3F80u;
      w[e] = lo | (hi_ << 16); }
    st16(srdK, (unsigned)(73728 + ((row0 >> 3) + cl) * 304 + d * 16), (u32x4){w[0], w[1], w[2], w[3]}); }
  asm volatile("s_waitcnt lgkmcnt(0)\n\ts_barrier" ::: "memory");
}
}
struct KArgs { const u16 *XA, *wqA, *wkA, *wvA, *woA, *XB, *Ckv, *WqB, *Wuk, *Wuv, *WoB; u16* out; char* ws; };
typedef const __attribute__((address_space(4))) KArgs* kargs_t;
DI kargs_t kargs() { kargs_t p = (kargs_t)__builtin_amdgcn_kernarg_segment_ptr(); asm volatile("" : "+s"(p)); return p; }
__global__ __launch_bounds__(512, 2) void k_mega(KArgs ka_unused) {
  extern __shared__ __attribute__((aligned(16))) char smem[];
  LAS char* lds = (LAS char*)smem;
  const int b = blockIdx.x, xcd = b & 7, idx = b >> 3;
  const int tid = threadIdx.x, wid = __builtin_amdgcn_readfirstlane(tid >> 6), lane = tid & 63;
  unsigned tag;
  { gu32* e = (gu32*)(kargs()->ws + WS_SYNC) + SY_EPOCH; tag = (unsigned)__builtin_amdgcn_readfirstlane(__hip_atomic_load(e, GRLX)) + 1u; asm volatile("" : "+s"(tag)); }
  CLK0();
  { char* ws0 = kargs()->ws; ST(ws0, 0, 0); }
  { kargs_t ka = kargs(); const u16 *XA = ka->XA, *wqA = ka->wqA, *wkA = ka->wkA, *wvA = ka->wvA; char* ws = ka->ws; gu32* sy = (gu32*)(ws + WS_SYNC);
  const unsigned long long pC = (unsigned long long)ka->Ckv, pK = (unsigned long long)ka->Wuk, pV = (unsigned long long)ka->Wuv;
  auto next = [=]() { g3::kv_first(lds, (const u16*)pC, (const u16*)((idx >> 3) < 2 ? pK : pV), idx & 7, 2 * xcd + ((idx >> 3) & 1)); };
  if ((idx & 7) == 7) {
    kva::tile(lds, 4 * xcd + (idx >> 3), XA, wkA, wvA, ws + WS_KA, next);
  } else {
    next();
  }
  {
    const unsigned spare = (unsigned)(uintptr_t)lds + 161792u + (unsigned)(wid & 1) * 1024u;
    attb::dma16(mk_srd(XA, 2048 * 1152), (unsigned)lane * 16u, (unsigned)((b * 9 + wid) * 1024), spare);
    if (wid < 4) { if (wid == 0) attb::dma16(mk_srd(XA, 2048 * 1152), (unsigned)lane * 16u, (unsigned)((b * 9 + 8) * 1024), spare);
                   else attb::dma16(mk_srd(wqA, 576 * 1152), (unsigned)lane * 16u, (unsigned)((b * 3 + wid - 1) * 1024), spare); }
  }
  ST(ws, 0, 1); }
  u32x4 resA0; unsigned resA1;
  { const int pm = idx & 7, j = idx >> 3, pnl = 2 * xcd + (j & 1);
    u32x32 pkA, pkB; unsigned lanebase;
    { kargs_t ka = kargs(); const u16 *Ckv = ka->Ckv, *Wuk = ka->Wuk, *Wuv = ka->Wuv; gu32* sy = (gu32*)(ka->ws + WS_SYNC);
    auto fl = [=]() { if (tid == 0) sy_set(sy + SY_G1 + b, tag); };
    const u16 *XA_ = ka->XA, *wqA_ = ka->wqA;
    auto al = [=]() { qa::proj<true>(lds, XA_, wqA_, (b & 7) + 8 * (b >> 5), (b >> 3) & 3); };
    if (j < 2) g3::kv_tile<true>(lds, Ckv, Wuk, pm, pnl, pkA, pkB, lanebase, fl, al);
    else       g3::kv_tile<false>(lds, Ckv, Wuv, pm, pnl, pkA, pkB, lanebase, fl, al); }
    kargs_t ka = kargs(); char* ws = ka->ws; gu32* sy = (gu32*)(ws + WS_SYNC); const u16 *XB = ka->XB, *WqB = ka->WqB, *WoB = ka->WoB, *woA = ka->woA;
    ST(ws, 0, 2);
    const int rb = (b & 7) + 8 * (b >> 5), hg = (b >> 3) & 3;
    if (wid == 0) { sy_wait<4>(sy + SY_G1, 256, tag, lane); sy_acquire(); if (b == 0 && lane == 0) sy_set(sy + SY_EPOCH, tag); }
    ST(ws, 0, 3);
    qa::proj<false>(lds, ka->XA, ka->wqA, rb, hg);
    const __amdgpu_buffer_rsrc_t srdO = mk_srd((ws + WS_KV), 32 * 32 * 32768);
    const unsigned spare = (unsigned)(uintptr_t)lds + 151552u + (unsigned)wid * 1024u;
    const bool isk = j < 2;
    const unsigned long long pXB = (unsigned long long)XB, pWq = (unsigned long long)WqB, pWoB = (unsigned long long)WoB, pWoA = (unsigned long long)woA;
    auto hook = [pkA, pkB, srdO, lanebase, isk, wid, lane, b, spare, pXB, pWq, pWoB, pWoA](auto I) { constexpr int i = I;
      if constexpr (i < 16) { const u32x32& p_ = i < 8 ? pkA : pkB; constexpr int e = 4 * (i & 7); g3::kv_store(srdO, lanebase, isk, i, (u32x4){p_[e], p_[e + 1], p_[e + 2], p_[e + 3]}); }
      if constexpr (i >= 16) {
        const int pc = wid + 8 * (i - 16);
        unsigned long long q0 = pXB, q1 = pWq, q2 = pWoB, q3 = pWoA;
        asm volatile("" : "+s"(q0), "+s"(q1), "+s"(q2), "+s"(q3));
        unsigned long long src_ = q3; int per = 3, o = pc - 45;
        if (pc < 45) { src_ = q2; per = 18; o = pc - 27; }
        if (pc < 27) { src_ = q1; o = pc - 9; }
        if (pc < 9) { src_ = q0; per = 9; o = pc; }
        const __amdgpu_buffer_rsrc_t sp = mk_srd_u((const void*)src_, per == 3 ? 576u * 1152u : (unsigned)(per * 256 * 1024));
        attb::dma16(sp, (unsigned)lane * 16u, (unsigned)((b * per + o) * 1024), spare);
      }
    };
    atta::run(lds, (const u16*)(ws + WS_QA), ws + WS_KA, ws + WS_OA, rb, hg, hook, resA0, resA1);
    asm volatile("s_waitcnt lgkmcnt(0)\n\ts_barrier" ::: "memory");
    if (tid == 0) sy_set(sy + SY_KV + pnl * 16 + (j >> 1) * 8 + pm, tag);
    ST(ws, 0, 4);
  }
  { const int head = 4 * xcd + (idx >> 3), x = idx & 7;
    kargs_t ka = kargs(); char* ws = ka->ws; gu32* sy = (gu32*)(ws + WS_SYNC); const u16 *XB = ka->XB, *WqB = ka->WqB;
    auto bg = [&](int T0, int NTH, int k3, auto&& ld) {
      using namespace attb;
      __builtin_amdgcn_s_setprio(3);
      for (int t = T0; t < NTH; ++t) {
        const int n = t - T0;
        const int lp = (t - 1 + PD < NTH) ? 8 - HP : 0;
        const int wn = n == 0 ? (T0 + 2 < NTH ? 4 : 0) + 8 : (n == 1 ? 8 + lp : lp);
        wait_bar_any(wn);
        if (t + PD < NTH) {
#pragma unroll
          for (int i = 0; i < 8 - HP; ++i) ld(t + PD, k3, i); }
        k3 = k3 == 2 ? 0 : k3 + 1;
      }
      __builtin_amdgcn_s_setprio(0);
    };
    auto kvwait = [&]() { if (wid == 0) { sy_wait<1>(sy + SY_KV + (head >> 1) * 16, 16, tag, lane); sy_acquire(); } };
    auto fl = [=]() { if (tid == 0) { const int pm = idx & 7, j = idx >> 3, pnl = 2 * xcd + (j & 1), rb = (b & 7) + 8 * (b >> 5), hg = (b >> 3) & 3;
      (void)pm; (void)j; (void)pnl; sy_set(sy + SY_OA + (rb >> 2) * 16 + (rb & 3) * 4 + hg, tag); } };
    auto post = [=]() { atta::store(ws + WS_OA, (b & 7) + 8 * (b >> 5), (b >> 3) & 3, resA0, resA1); };
    attb::run(lds, XB, WqB, ws + WS_KV, ws + WS_CTX, head, x, bg, kvwait, fl, post);
    asm volatile("s_waitcnt vmcnt(0) lgkmcnt(0)\n\ts_barrier" ::: "memory");
    if (tid == 0) { sy_release(); sy_set(sy + SY_CTX + (15 - x) * 32 + head, tag); sy_set(sy + SY_CTX + x * 32 + head, tag); }
    ST(ws, 0, 5);
  }
  {
    const int x_ = idx & 7, hh = idx >> 3; const bool s3 = x_ == 5 || x_ == 6;
    const int u_ = hh * 6 + (x_ == 7 ? 5 : x_), tl = s3 ? hh * 2 + (x_ - 5) : u_ / 3, s = s3 ? 3 : u_ - 3 * (u_ / 3);
    const int ti = 2 * xcd + (tl >> 2), tj = tl & 3;
    kargs_t ka = kargs(); char* ws = ka->ws; gu32* sy = (gu32*)(ws + WS_SYNC); const u16 *WoB = ka->WoB, *woA = ka->woA; u16* out = ka->out;
    ST(ws, 0, 6);
    if (wid < 4) outp::body<false>(lds, ws, WoB, woA, out, ti, tj, s, sy, tag);
    else outp::body<true>(lds, ws, WoB, woA, out, ti, tj, s, sy, tag);
    ST(ws, 0, 7);
    CLK1(ws, 0);
  }
}
extern "C" void kernel_launch(void* const* d_in, const int* in_sizes, int n_in, void* d_out, int out_size, void* d_ws, size_t ws_size, hipStream_t stream) {
  const u16 *Ax = (const u16*)d_in[0], *Awq = (const u16*)d_in[1], *Awk = (const u16*)d_in[2], *Awv = (const u16*)d_in[3], *Awo = (const u16*)d_in[4];
  const u16 *Bx = (const u16*)d_in[5], *Bc = (const u16*)d_in[6], *Bwq = (const u16*)d_in[7], *Bwuk = (const u16*)d_in[8], *Bwuv = (const u16*)d_in[9], *Bwo = (const u16*)d_in[10];
  char* ws = (char*)d_ws;
  (void)hipFuncSetAttribute((const void*)k_mega, hipFuncAttributeMaxDynamicSharedMemorySize, 160 * 1024);
  KArgs ka{Ax, Awq, Awk, Awv, Awo, Bx, Bc, Bwq, Bwuk, Bwuv, Bwo, (u16*)d_out, ws};
  k_mega<<<256, 512, 160 * 1024, stream>>>(ka);
}
```
